```python
import torch

def _softmax(x, axis=-1):
    return torch.softmax(x, dim=axis)
def _gelu(x):
    return torch.nn.functional.gelu(x, approximate="tanh")
def _silu(x):
    return torch.nn.functional.silu(x)
def _sigmoid(x):
    return torch.sigmoid(x)
def _layernorm(x, axis=-1, eps=1e-5):
    m = x.mean(dim=axis, keepdim=True)
    v = x.var(dim=axis, keepdim=True, unbiased=False)
    return (x - m) / torch.sqrt(v + eps)
def _scatter_add(tgt, idx, src, axis=0):
    return tgt.clone().index_add_(axis, idx, src)
def _segment_sum(src, seg, n):
    out = torch.zeros((n,) + src.shape[1:], dtype=src.dtype)
    return out.index_add_(0, seg, src)
def _pad(x, pw):
    flat = tuple(v for pair in reversed(pw) for v in pair)
    return torch.nn.functional.pad(x, flat)

kernel_name = 'parallel_sum__grouped_kv_mqa__dense_activation_squared_relu__prompt'

def setup_inputs():

    _w = (lambda a: (lambda t: t.to(torch.bfloat16) if t.is_floating_point() else t)(torch.as_tensor(a)))
    def _A_setup():
        B, S, D, H = 1, 2048, 576, 32
        Dh = D // H
        return {
            "x": _w((lambda *s: torch.randn(*s, dtype=torch.bfloat16) * 0.05)(B, S, D)),
            "wq": _w((lambda *s: torch.randn(*s, dtype=torch.bfloat16) * 0.2)(D, H * Dh)),
            "wk": _w((lambda *s: torch.randn(*s, dtype=torch.bfloat16) * 0.2)(D, Dh)),
            "wv": _w((lambda *s: torch.randn(*s, dtype=torch.bfloat16) * 0.1)(D, Dh)),
            "wo": _w((lambda *s: torch.randn(*s, dtype=torch.bfloat16) * 0.1)(H * Dh, D)),
        }
    _A_in = _A_setup()

    def _B_setup():
        B, S, D, F = 1, 2048, 576, 4608
        return {
            "x": _w((lambda *s: torch.randn(*s, dtype=torch.bfloat16) * 0.05)(B, S, D)),
            "w1": _w((lambda *s: torch.randn(*s, dtype=torch.bfloat16) * 0.05)(D, F)),
            "b1": _w((lambda *s: torch.randn(*s, dtype=torch.bfloat16) * 0.8)(F)),
            "w2": _w((lambda *s: torch.randn(*s, dtype=torch.bfloat16) * 0.05)(F, D)),
            "b2": _w((lambda *s: torch.randn(*s, dtype=torch.bfloat16) * 12.8)(D)),
        }
    _B_in = _B_setup()

    return {
        "A_x": _A_in["x"],
        "A_wq": _A_in["wq"],
        "A_wk": _A_in["wk"],
        "A_wv": _A_in["wv"],
        "A_wo": _A_in["wo"],
        "B_x": _B_in["x"],
        "B_w1": _B_in["w1"],
        "B_b1": _B_in["b1"],
        "B_w2": _B_in["w2"],
        "B_b2": _B_in["b2"],
    }

def reference(A_x, A_wq, A_wk, A_wv, A_wo, B_x, B_w1, B_b1, B_w2, B_b2):
    def _A_ref(x, wq, wk, wv, wo):
        B, S, D = x.shape
        H = 32
        Dh = D // H
        q = (lambda x, *d: x.permute(*d))((x @ wq).reshape(B, S, H, Dh), 0, 2, 1, 3)
        k = x @ wk
        v = x @ wv
        scores = torch.einsum("bhqd,bkd->bhqk", q, k) * (Dh ** -0.5)
        p = _softmax(scores, axis=-1)
        o = torch.einsum("bhqk,bkd->bhqd", p, v)
        return (lambda x, *d: x.permute(*d))(o, 0, 2, 1, 3).reshape(B, S, H * Dh) @ wo

    def _B_ref(x, w1, b1, w2, b2):
        h = torch.einsum("bsd,df->bsf", x, w1) + b1
        zero = torch.zeros_like(h)
        r = torch.where(h > zero, h, zero)
        h = r * r
        out = torch.einsum("bsf,fd->bsd", h, w2) + b2
        return out

    x = A_x
    xn = _layernorm(x, axis=-1)
    return x + _A_ref(xn, A_wq, A_wk, A_wv, A_wo) + _B_ref(xn, B_w1, B_b1, B_w2, B_b2)
```

```cpp
#include <hip/hip_runtime.h>
#include <hip/hip_bf16.h>
#include <stdint.h>
typedef unsigned short u16;
__device__ __forceinline__ float bf2f(u16 v) { return __uint_as_float(((unsigned)v) << 16); }
__device__ __forceinline__ u16 f2bf(float f) { unsigned u = __float_as_uint(f); return (u16)((u + 0x7FFF + ((u >> 16) & 1)) >> 16); }
__device__ __forceinline__ float rb(float f) { return bf2f(f2bf(f)); }
constexpr int S = 2048, D = 576, H = 32, DH = 18, F = 4608;

__global__ void k_ln(const u16* x, float* xn) {
  int row = blockIdx.x, l = threadIdx.x;
  float v[9]; float s = 0;
  for (int i = 0; i < 9; i++) { v[i] = bf2f(x[row * D + l + 64 * i]); s += v[i]; }
  for (int o = 32; o; o >>= 1) s += __shfl_xor(s, o);
  float mean = s / D; float q = 0;
  for (int i = 0; i < 9; i++) q += (v[i] - mean) * (v[i] - mean);
  for (int o = 32; o; o >>= 1) q += __shfl_xor(q, o);
  float var = rb(q / D); float sd = rb(sqrtf(rb(var + 1e-5f))); float mb = rb(mean);
  for (int i = 0; i < 9; i++) xn[row * D + l + 64 * i] = rb(v[i] - mb) / sd;
}
__global__ void k_gemm(const float* A, const u16* W, float* C, int M, int N, int K, int mode, float scale, const u16* bias) {
  int n = blockIdx.x * 64 + threadIdx.x; int m0 = blockIdx.y * 8;
  if (n >= N) return;
  float acc[8] = {0};
  for (int k = 0; k < K; k++) { float w = bf2f(W[(size_t)k * N + n]);
    for (int i = 0; i < 8; i++) acc[i] += A[(size_t)(m0 + i) * K + k] * w; }
  for (int i = 0; i < 8; i++) { float v = acc[i];
    if (mode == 0) v = rb(v * scale); else if (mode == 1) { v += bf2f(bias[n]); v = v > 0 ? v : 0; v = rb(v * v); }
    C[(size_t)(m0 + i) * N + n] = v; }
}
__global__ void k_attn(const float* q, const float* k, const float* v, float* o) {
  int idx = blockIdx.x * 64 + threadIdx.x; int qi = idx / H, h = idx % H;
  float qq[DH]; for (int d = 0; d < DH; d++) qq[d] = q[qi * D + h * DH + d];
  float m = -INFINITY;
  for (int j = 0; j < S; j++) { float s = 0; for (int d = 0; d < DH; d++) s += qq[d] * k[j * DH + d]; m = fmaxf(m, s); }
  float l = 0, acc[DH] = {0};
  for (int j = 0; j < S; j++) { float s = 0; for (int d = 0; d < DH; d++) s += qq[d] * k[j * DH + d];
    float p = rb(exp2f(s - m)); l += p; for (int d = 0; d < DH; d++) acc[d] += p * v[j * DH + d]; }
  for (int d = 0; d < DH; d++) o[qi * D + h * DH + d] = rb(acc[d] / l);
}
__global__ void k_final(const u16* x, const float* a, const float* b, const u16* b2, u16* out) {
  int i = blockIdx.x * 256 + threadIdx.x; out[i] = f2bf(bf2f(x[i]) + a[i] + b[i] + bf2f(b2[i % D]));
}
extern "C" void kernel_launch(void* const* d_in, const int* in_sizes, int n_in, void* d_out, int out_size, void* d_ws, size_t ws_size, hipStream_t stream) {
  const u16* x = (const u16*)d_in[0]; const u16* wq = (const u16*)d_in[1]; const u16* wk = (const u16*)d_in[2]; const u16* wv = (const u16*)d_in[3]; const u16* wo = (const u16*)d_in[4];
  const u16* w1 = (const u16*)d_in[6]; const u16* b1 = (const u16*)d_in[7]; const u16* w2 = (const u16*)d_in[8]; const u16* b2 = (const u16*)d_in[9];
  float* ws = (float*)d_ws;
  float* xn = ws; float* q = xn + S * D; float* k = q + S * D; float* v = k + S * 32; float* o = v + S * 32; float* a = o + S * D; float* b = a + S * D; float* hh = b + S * D;
  k_ln<<<S, 64, 0, stream>>>(x, xn);
  float c = 0.23570226039551584f * 1.4426950408889634f;
  k_gemm<<<dim3(9, S / 8), 64, 0, stream>>>(xn, wq, q, S, D, D, 0, c, nullptr);
  k_gemm<<<dim3(1, S / 8), 64, 0, stream>>>(xn, wk, k, S, DH, D, 0, 1.f, nullptr);
  k_gemm<<<dim3(1, S / 8), 64, 0, stream>>>(xn, wv, v, S, DH, D, 0, 1.f, nullptr);
  k_gemm<<<dim3(F / 64, S / 8), 64, 0, stream>>>(xn, w1, hh, S, F, D, 1, 1.f, b1);
  k_attn<<<S * H / 64, 64, 0, stream>>>(q, k, v, o);
  k_gemm<<<dim3(9, S / 8), 64, 0, stream>>>(o, wo, a, S, D, D, 2, 1.f, nullptr);
  k_gemm<<<dim3(9, S / 8), 64, 0, stream>>>(hh, w2, b, S, D, F, 2, 1.f, nullptr);
  k_final<<<S * D / 256, 256, 0, stream>>>(x, a, b, b2, (u16*)d_out);
}
```

```cpp
#include <hip/hip_runtime.h>
#include <stdint.h>

typedef unsigned short u16;
typedef unsigned int u32;
typedef __bf16 bf16x8 __attribute__((ext_vector_type(8)));
typedef __bf16 bf16x2 __attribute__((ext_vector_type(2)));
typedef float f32x2 __attribute__((ext_vector_type(2)));
typedef float f32x4 __attribute__((ext_vector_type(4)));
typedef float f32x16 __attribute__((ext_vector_type(16)));
typedef short s16x4 __attribute__((ext_vector_type(4)));
typedef u32 u32x4 __attribute__((ext_vector_type(4)));
typedef u32 u32x2 __attribute__((ext_vector_type(2)));
#define LAS __attribute__((address_space(3)))
#define DI __device__ __forceinline__
#define SBAR() __builtin_amdgcn_sched_barrier(0)
#define PIN(x) asm volatile("" : "+v"(x))

constexpr int S = 2048, D = 576, F = 4608;
constexpr float QSCALE = 0.23570226039551584f * 1.4426950408889634f;

constexpr size_t WS_XF  = 0;
constexpr size_t WS_WKV = WS_XF + (size_t)64 * 36 * 1024;
constexpr size_t WS_W2T = WS_WKV + (size_t)576 * 64 * 2;
constexpr size_t WS_WOT = WS_W2T + (size_t)18 * 288 * 1024;
constexpr size_t WS_HF  = WS_WOT + (size_t)36 * 18 * 1024;
constexpr size_t WS_QF  = WS_HF + (size_t)64 * 288 * 1024;
constexpr size_t WS_KV  = WS_QF + (size_t)64 * 36 * 1024;
constexpr int KVT = 2368;
constexpr size_t WS_OF  = WS_KV + (size_t)64 * KVT;
constexpr size_t WS_G   = WS_OF + (size_t)128 * 18 * 1024;

DI float bf2f(u32 v) { return __uint_as_float(v << 16); }
DI u32 pk(float a, float b) { f32x2 v = {a, b}; return __builtin_bit_cast(u32, __builtin_convertvector(v, bf16x2)); }
DI float rbf(float a) { return __uint_as_float(pk(a, 0.f) << 16); }
DI f32x16 mfma32(u32x4 a, u32x4 b, f32x16 c) { return __builtin_amdgcn_mfma_f32_32x32x16_bf16(__builtin_bit_cast(bf16x8, a), __builtin_bit_cast(bf16x8, b), c, 0, 0, 0); }
DI f32x4 mfma16(u32x4 a, u32x4 b, f32x4 c) { return __builtin_amdgcn_mfma_f32_16x16x32_bf16(__builtin_bit_cast(bf16x8, a), __builtin_bit_cast(bf16x8, b), c, 0, 0, 0); }
DI void dma16(const void* sbase, u32 voff, u32 ldsaddr) {
  asm volatile("s_mov_b32 m0, %2\n\ts_nop 0\n\tglobal_load_lds_dwordx4 %0, %1" :: "v"(voff), "s"(sbase), "s"(ldsaddr) : "memory", "m0");
}
DI u32x2 trread(LAS const char* p) { return __builtin_bit_cast(u32x2, __builtin_amdgcn_ds_read_tr16_b64_v4i16((LAS s16x4*)p)); }
#define STAMP(kid, i)
#define WAITV(n) asm volatile("s_waitcnt vmcnt(" #n ")" ::: "memory")

__global__ __launch_bounds__(256) void k0_prep(const u16* __restrict__ x, const u16* __restrict__ wk, const u16* __restrict__ wv,
                                               const u16* __restrict__ wo, const u16* __restrict__ w2, char* __restrict__ ws) {
  const int lane = threadIdx.x & 63, wave = __builtin_amdgcn_readfirstlane(threadIdx.x >> 6);
  STAMP(0, 0);
  if (wave == 0) {
    const int m = blockIdx.x * 8 + (lane >> 3), c0 = lane & 7;
    u32x4 v[9];
#pragma unroll
    for (int i = 0; i < 9; i++) v[i] = *(const u32x4*)(x + (size_t)m * D + (c0 + 8 * i) * 8);
    float s = 0.f;
#pragma unroll
    for (int i = 0; i < 9; i++)
#pragma unroll
      for (int e = 0; e < 4; e++) s += bf2f(v[i][e] & 0xffffu) + __uint_as_float(v[i][e] & 0xffff0000u);
    s += __shfl_xor(s, 1); s += __shfl_xor(s, 2); s += __shfl_xor(s, 4);
    const float mean = s * (1.0f / D);
    float q = 0.f;
#pragma unroll
    for (int i = 0; i < 9; i++)
#pragma unroll
      for (int e = 0; e < 4; e++) { float a = bf2f(v[i][e] & 0xffffu) - mean, b = __uint_as_float(v[i][e] & 0xffff0000u) - mean; q += a * a + b * b; }
    q += __shfl_xor(q, 1); q += __shfl_xor(q, 2); q += __shfl_xor(q, 4);
    const float var = rbf(q * (1.0f / D)), sd = rbf(sqrtf(rbf(var + 1e-5f))), mb = rbf(mean);
#pragma unroll
    for (int i = 0; i < 9; i++) {
      u32x4 o;
#pragma unroll
      for (int e = 0; e < 4; e++) {
        u32 c = pk(bf2f(v[i][e] & 0xffffu) - mb, __uint_as_float(v[i][e] & 0xffff0000u) - mb);
        o[e] = pk(bf2f(c & 0xffffu) / sd, __uint_as_float(c & 0xffff0000u) / sd);
      }
      const int ch = c0 + 8 * i;
      *(u32x4*)(ws + WS_XF + ((size_t)((m >> 5) * 36 + (ch >> 1)) * 64 + (ch & 1) * 32 + (m & 31)) * 16) = o;
    }
  }
  for (int t = blockIdx.x * 4 + wave; t < 5184 + 648 + 72; t += 1024) {
    u16 e[8];
    char* dst;
    if (t < 5184) {
      const int k16 = t / 18, nt = t % 18, r = lane & 31, h = lane >> 5;
#pragma unroll
      for (int j = 0; j < 8; j++) e[j] = w2[(size_t)(16 * k16 + 8 * (j >> 2) + 4 * h + (j & 3)) * D + 32 * nt + r];
      dst = ws + WS_W2T + ((size_t)(nt * 288 + k16) * 64 + lane) * 16;
    } else if (t < 5184 + 648) {
      const int u = t - 5184, ks = u / 36, nt = u % 36, c = lane & 15, ch = 4 * ks + (lane >> 4), gh = ch / 9, i = ch % 9;
#pragma unroll
      for (int j = 0; j < 8; j++) {
        const int k = (i < 8) ? 18 * (4 * gh + (i >> 1)) + 8 * (j >> 2) + 4 * (i & 1) + (j & 3) : 18 * (4 * gh + (j >> 1)) + 16 + (j & 1);
        e[j] = wo[(size_t)k * D + 16 * nt + c];
      }
      dst = ws + WS_WOT + ((size_t)(nt * 18 + ks) * 64 + lane) * 16;
    } else {
      const int u = t - 5184 - 648, row = u * 8 + (lane >> 3), c0 = (lane & 7) * 8;
#pragma unroll
      for (int j = 0; j < 8; j++) { const int c = c0 + j, cc = c & 31; const u16* src = c < 32 ? wk : wv; e[j] = cc < 18 ? src[row * 18 + cc] : (u16)0; }
      dst = ws + WS_WKV + ((size_t)u * 64 + lane) * 16;
    }
    u32x4 o = {e[0] | ((u32)e[1] << 16), e[2] | ((u32)e[3] << 16), e[4] | ((u32)e[5] << 16), e[6] | ((u32)e[7] << 16)};
    *(u32x4*)dst = o;
  }
  STAMP(0, 4);
}

constexpr int K1_STAGE = 24576, K1_BIAS = 6 * K1_STAGE;
constexpr int K1_LDS = K1_BIAS + 320 * 4;

__global__ __launch_bounds__(256, 1) void k1_gemm(const u16* __restrict__ w1, const u16* __restrict__ wq, const u16* __restrict__ b1, char* __restrict__ ws) {
  __shared__ __attribute__((aligned(16))) char lds[K1_LDS];
  const int tid = threadIdx.x, lane = tid & 63, wave = __builtin_amdgcn_readfirstlane(tid >> 6);
  const int r = lane & 31, h = lane >> 5;
  const int xcd = blockIdx.x & 7, idx = blockIdx.x >> 3;
  const int mg = (xcd & 1) * 8 + (idx & 7), g = (xcd >> 1) * 4 + (idx >> 3);
  const int mt = mg * 4 + wave;
  const int hp0 = g < 8 ? 4 * g : 32 + 5 * (g - 8);
  const int nH = g < 8 ? 4 : 5;
  const bool has_single = g >= 12;
  const int TS = has_single ? 18 : 15;
  const u32 ldsb = (u32)(uintptr_t)(LAS char*)lds;
  const u32 drow = (lane & 31) >> 2, dch = (lane & 3) * 16, dt = lane >> 5;
  const char* spbase = g < 8 ? (const char*)wq + g * 128 : g < 14 ? (const char*)wq + (16 + g - 12) * 64 : ws + WS_WKV + (g - 14) * 64;
  const u32 spld = g < 14 ? D * 2 : 128;
  const u32 voffH = drow * (F * 2) + dch + dt * 64, voffS = drow * spld + dch + (g < 8 ? dt * 64 : 0);
  const char* hbase = (const char*)w1 + hp0 * 128 + (u32)wave * (48u * F * 2);
  spbase += (u32)wave * 48u * spld;
  const u32 ldsw = ldsb + wave * 6144;
#define DMA_ST(p, c, sl, j) do { \
    if ((p) < nH) dma16(hbase + ((p) * 128 + (192u * (c) + 8u * (j)) * (F * 2)), voffH, ldsw + (sl) * K1_STAGE + (j) * 1024); \
    else dma16(spbase + (192u * (c) + 8u * (j)) * spld, voffS, ldsw + (sl) * K1_STAGE + (j) * 1024); } while (0)
  STAMP(1, 0);
  u32x4 xf[36];
  const char* xsrc = ws + WS_XF + (size_t)mt * 36 * 1024 + lane * 16;
#pragma unroll
  for (int j = 0; j < 6; j++) DMA_ST(0, 0, 0, j);
#pragma unroll
  for (int s = 0; s < 36; s++) xf[s] = *(const u32x4*)(xsrc + s * 1024);
  for (int i = tid; i < 320; i += 256) ((LAS float*)(lds + K1_BIAS))[i] = (i < nH * 64) ? bf2f(b1[hp0 * 64 + i]) : 0.f;
#pragma unroll
  for (int q = 1; q < 5; q++)
#pragma unroll
    for (int j = 0; j < 6; j++) DMA_ST(q / 3, q % 3, q, j);
  asm volatile("s_waitcnt vmcnt(24) lgkmcnt(0)\n\ts_barrier" ::: "memory");
  const int lpart = h * 1024 + ((lane & 15) >> 2) * 64 + ((lane >> 4) & 1) * 32 + (lane & 3) * 8;
  LAS const char* lb = (LAS const char*)lds + lpart;
  LAS const char* bb = (LAS const char*)lds + K1_BIAS + h * 16;
  u32x2 wf[3][2][2];
#define WREAD(base, st, NT) do { _Pragma("unroll") for (int t_ = 0; t_ < NT; t_++) { \
    wf[(st) % 3][t_][0] = trread((base) + ((st) % 36 / 12) * K1_STAGE + ((st) % 12) * 2048 + t_ * 512); \
    wf[(st) % 3][t_][1] = trread((base) + ((st) % 36 / 12) * K1_STAGE + ((st) % 12) * 2048 + t_ * 512 + 256); } } while (0)
#define WFRAG(st, t) (u32x4){wf[(st) % 3][t][0][0], wf[(st) % 3][t][0][1], wf[(st) % 3][t][1][0], wf[(st) % 3][t][1][1]}
  WREAD(lb, 0, 2); WREAD(lb, 1, 2);
  char* hdst = ws + WS_HF + ((size_t)mt * 288 + hp0 * 4) * 1024 + lane * 16;
  f32x16 A0, A1, B0, B1;
#pragma unroll
  for (int i = 0; i < 16; i++) { B0[i] = 0.f; B1[i] = 0.f; }
#define BODY(C0, C1, P0, P1, p, PAR, NT) do { \
    LAS const char* cur_ = lb + (PAR) * 3 * K1_STAGE; LAS const char* nxt_ = lb + (1 - (PAR)) * 3 * K1_STAGE; \
    LAS const char* bp_ = bb + ((p) - 1) * 256; \
    f32x4 bias_[2]; u32x4 ov_; \
    if ((p) > 0) bias_[0] = *(LAS const f32x4*)(bp_); else bias_[0] = (f32x4){0.f, 0.f, 0.f, 0.f}; \
    _Pragma("unroll") for (int s = 0; s < 36; s++) { \
      const int q_ = 3 * (p) + s / 12; \
      if (s % 12 == 10) { \
        const int n_ = TS - 2 - q_; \
        if (n_ >= 3) WAITV(18); else if (n_ == 2) WAITV(12); else if (n_ == 1) WAITV(6); else WAITV(4); \
        __builtin_amdgcn_s_barrier(); SBAR(); \
      } \
      { const int dq_ = s / 12 + ((s % 12 >= 10) ? 5 : 4), j_ = (s % 12 >= 10) ? s % 12 - 10 : s % 12 + 2; \
        if (j_ < 6 && 3 * (p) + dq_ >= 5 && 3 * (p) + dq_ < TS) DMA_ST((p) + dq_ / 3, dq_ % 3, (3 * (PAR) + dq_) % 6, j_); } \
      if (s + 2 < 36) WREAD(cur_, s + 2, NT); else WREAD(nxt_, s + 2, 2); \
      if (s < 16 && (s & 1) == 0 && s + 2 < 16) { if ((p) > 0) bias_[((s >> 1) + 1) & 1] = *(LAS const f32x4*)(bp_ + ((s + 2) >> 3) * 128 + (((s + 2) >> 1) & 3) * 32); } \
      if (s == 0) { f32x16 z_; _Pragma("unroll") for (int i = 0; i < 16; i++) z_[i] = 0.f; \
        C0 = mfma32(WFRAG(s, 0), xf[s], z_); if (NT == 2) C1 = mfma32(WFRAG(s, 1), xf[s], z_); } \
      else { C0 = mfma32(WFRAG(s, 0), xf[s], C0); if (NT == 2) C1 = mfma32(WFRAG(s, 1), xf[s], C1); } \
      if (s < 16) { \
        const int e_ = (2 * s) & 15; \
        float a_ = (s < 8 ? P0[e_] : P1[e_]) + bias_[(s >> 1) & 1][e_ & 3], b_ = (s < 8 ? P0[e_ + 1] : P1[e_ + 1]) + bias_[(s >> 1) & 1][(e_ & 3) + 1]; \
        a_ = fmaxf(a_, 0.f); b_ = fmaxf(b_, 0.f); \
        ov_[s & 3] = pk(a_ * a_, b_ * b_); PIN(ov_); \
        if ((s & 3) == 3 && (p) > 0) *(u32x4*)(hdst + ((p) - 1) * 4096 + (s >> 2) * 1024) = ov_; \
      } \
      SBAR(); \
    } } while (0)
  STAMP(1, 1);
  for (int p = 0; ; p += 2) {
    BODY(A0, A1, B0, B1, p, 0, 2);
    if (p == 4) break;
    BODY(B0, B1, A0, A1, p + 1, 1, 2);
  }
  STAMP(1, 3);
  auto store_h = [&](const f32x16& P, int t, int p) {
#pragma unroll
    for (int s = 0; s < 2; s++) {
      u32x4 o;
#pragma unroll
      for (int e = 0; e < 2; e++) {
        const f32x4 b = *(LAS const f32x4*)(bb + p * 256 + t * 128 + (2 * s + e) * 32);
#pragma unroll
        for (int i = 0; i < 2; i++) {
          float a0 = fmaxf(P[8 * s + 4 * e + 2 * i] + b[2 * i], 0.f), a1 = fmaxf(P[8 * s + 4 * e + 2 * i + 1] + b[2 * i + 1], 0.f);
          o[2 * e + i] = pk(a0 * a0, a1 * a1);
        }
      }
      *(u32x4*)(hdst + p * 4096 + (2 * t + s) * 1024) = o;
    }
  };
  auto store_q = [&](const f32x16& P, int qt) {
#pragma unroll
    for (int s = 0; s < 2; s++) {
      u32x4 o;
#pragma unroll
      for (int i = 0; i < 4; i++) { const u32 c = pk(P[8 * s + 2 * i], P[8 * s + 2 * i + 1]); o[i] = pk(bf2f(c & 0xffffu) * QSCALE, __uint_as_float(c & 0xffff0000u) * QSCALE); }
      *(u32x4*)(ws + WS_QF + ((size_t)(mt * 36 + 2 * qt + s) * 64 + lane) * 16) = o;
    }
  };
  if (g < 8) { store_q(A0, 2 * g); store_q(A1, 2 * g + 1); }
  else if (!has_single) { store_h(A0, 0, 4); store_h(A1, 1, 4); }
  else {
    BODY(B0, B1, A0, A1, 5, 1, 1);
    char* kv = ws + WS_KV + (size_t)mt * KVT;
    if (g < 14) store_q(B0, 16 + g - 12);
    else if (g == 14) {
      u32x4 o = {pk(B0[0], B0[1]), pk(B0[2], B0[3]), pk(B0[4], B0[5]), pk(B0[6], B0[7])};
      *(u32x4*)(kv + lane * 16) = o;
      if (h == 0) *(u32*)(kv + 1024 + r * 4) = pk(B0[8], B0[9]);
    } else {
      u16* vb = (u16*)(kv + 1152) + (((r >> 4) * 2 + ((r >> 2) & 1)) * 19) * 8 + ((r >> 3) & 1) * 4 + (r & 3);
#pragma unroll
      for (int i = 0; i < 8; i++) vb[((i & 3) + 8 * (i >> 2) + 4 * h) * 8] = (u16)(pk(B0[i], 0.f) & 0xffffu);
      if (h == 0) { vb[16 * 8] = (u16)(pk(B0[8], 0.f) & 0xffffu); vb[17 * 8] = (u16)(pk(B0[9], 0.f) & 0xffffu); vb[18 * 8] = (u16)0x3f80; }
    }
  }
  STAMP(1, 4);
}

constexpr int K2_KV = 64 * KVT, K2_LDS = 163840;
constexpr float M_MARGIN = 80.f, L_TRIG = 1024.f;

__global__ __launch_bounds__(512, 2) void k2_attn_gemm(char* __restrict__ ws) {
  __shared__ __attribute__((aligned(16))) char lds[K2_LDS];
  const int tid = threadIdx.x, lane = tid & 63, wave = __builtin_amdgcn_readfirstlane(tid >> 6);
  const int r = lane & 31, h = lane >> 5;
  const u32 ldsb = (u32)(uintptr_t)(LAS char*)lds;
  const int xcd = blockIdx.x & 7, idx = blockIdx.x >> 3;
  STAMP(2, 0);
  for (int i = wave; i < 148; i += 8) dma16(ws + WS_KV + i * 1024, lane * 16, ldsb + i * 1024);
  if (wave < 4) {
    const int qb = blockIdx.x >> 3, hg = blockIdx.x & 7, head = hg * 4 + wave;
    u32x4 qm[2], qt[2];
#pragma unroll
    for (int t = 0; t < 2; t++) {
      const char* qsrc = ws + WS_QF + (size_t)(qb * 2 + t) * 36 * 1024 + r * 16;
      u32 w[5];
#pragma unroll
      for (int i = 0; i < 5; i++) {
        const int col = head * 18 + (i < 4 ? 8 * (i >> 1) + 4 * h + 2 * (i & 1) : 16), cc = col & 15;
        w[i] = *(const u32*)(qsrc + (col >> 4) * 1024 + ((cc >> 2) & 1) * 512 + ((cc >> 3) * 4 + (cc & 3)) * 2);
      }
      qm[t] = (u32x4){w[0], w[1], w[2], w[3]};
      qt[t] = (u32x4){h == 0 ? w[4] : 0u, 0u, 0u, 0u};
    }
    WAITV(0);
    __builtin_amdgcn_s_barrier();
    STAMP(2, 1);
    const int vr = r < 19 ? r : (r == 22 ? 18 : r - 16);
    const int koff = lane * 16, toff = 1024 + r * 4, voff = 1152 + (h * 19 + vr) * 16;
    f32x16 zero;
#pragma unroll
    for (int i = 0; i < 16; i++) zero[i] = 0.f;
    f32x16 oA, oB;
    float mA, mB;
    auto set_ref = [&](float want, u32x4& qtail) -> float {
      const float hi = rbf(want), lo = rbf(want - hi);
      qtail[1] = h == 0 ? pk(-hi, -lo) : 0u;
      return hi + lo;
    };
    {
      LAS const char* kp = (LAS const char*)lds;
      u32x4 km = *(LAS const u32x4*)(kp + koff);
      u32x4 kt = {*(LAS const u32*)(kp + toff), 0x3f803f80u, 0u, 0u};
      f32x16 sA = mfma32(kt, qt[0], mfma32(km, qm[0], zero)), sB = mfma32(kt, qt[1], mfma32(km, qm[1], zero));
      float a = sA[0], b = sB[0];
#pragma unroll
      for (int i = 1; i < 16; i++) { a = fmaxf(a, sA[i]); b = fmaxf(b, sB[i]); }
      a = fmaxf(a, __shfl_xor(a, 32)); b = fmaxf(b, __shfl_xor(b, 32));
      mA = set_ref(ceilf(a) + M_MARGIN, qt[0]); mB = set_ref(ceilf(b) + M_MARGIN, qt[1]);
    }
    f32x16 s0A, s0B, s1A, s1B;
    u32x4 pA0, pA1, pB0, pB1, km, kt, v0, v1;
    LAS const char* kp = (LAS const char*)lds + koff;
    LAS const char* tp = (LAS const char*)lds + toff;
    LAS const char* vp = (LAS const char*)lds + voff;
    kt = (u32x4){0u, 0x3f803f80u, 0u, 0u};
    km = *(LAS const u32x4*)(kp); kt[0] = *(LAS const u32*)(tp);
    s0A = mfma32(kt, qt[0], mfma32(km, qm[0], zero)); s0B = mfma32(kt, qt[1], mfma32(km, qm[1], zero));
    km = *(LAS const u32x4*)(kp + KVT); kt[0] = *(LAS const u32*)(tp + KVT);
    v0 = *(LAS const u32x4*)(vp); v1 = v0;
    pA0 = (u32x4){0u, 0u, 0u, 0u}; pA1 = pA0; pB0 = pA0; pB1 = pA0;
    oA = zero; oB = zero;
#define XE(B, i) "v_exp_f32 v[" #B "+" #i "], v[" #B "+" #i "]\n\t"
#define XE3(B, i) XE(B, i) XE(B, i + 1) XE(B, i + 2)
#define XC(P, B, i) "v_cvt_pk_bf16_f32 v[" #P "], v[" #B "+" #i "], v[" #B "+" #i "+1]\n\t"
#define XM(D, A, Bo, C) "v_mfma_f32_32x32x16_bf16 v[" #D ":" #D "+15], v[" #A ":" #A "+3], v[" #Bo ":" #Bo "+3], " C "\n\t"
#define XR(D) "v[" #D ":" #D "+15]"
#define XSTEP(CA, CB, NA, NB, OFF) \
      "s_waitcnt lgkmcnt(0)\n\t" \
      XM(0, 136, 96, XR(0)) XE3(CA, 0) XM(16, 136, 104, XR(16)) XE3(CA, 3) XM(0, 140, 100, XR(0)) XE3(CA, 6) XM(16, 140, 108, XR(16)) XE3(CA, 9) \
      XM(NA, 128, 112, "0") XE3(CA, 12) XM(NB, 128, 116, "0") XE(CA, 15) XE(CB, 0) XE(CB, 1) XM(NA, 132, 120, XR(NA)) XE3(CB, 2) XM(NB, 132, 124, XR(NB)) \
      XE(CB, 5) "ds_read_b128 v[136:139], v146 offset:" #OFF "\n\t" XE(CB, 6) "ds_read_b128 v[140:143], v146 offset:" #OFF "+608\n\t" \
      XE(CB, 7) "ds_read_b128 v[128:131], v144 offset:" #OFF "+4736\n\t" XE(CB, 8) "ds_read_b32 v132, v145 offset:" #OFF "+4736\n\t" \
      XE3(CB, 9) XE3(CB, 12) XE(CB, 15) \
      XC(96, CA, 0) XC(97, CA, 2) XC(98, CA, 4) XC(99, CA, 6) XC(104, CB, 0) XC(105, CB, 2) XC(106, CB, 4) XC(107, CB, 6) \
      XC(100, CA, 8) XC(101, CA, 10) XC(102, CA, 12) XC(103, CA, 14) XC(108, CB, 8) XC(109, CB, 10) XC(110, CB, 12) XC(111, CB, 14)
    int t = 0;
    u32 kpa = (u32)(uintptr_t)kp, tpa = (u32)(uintptr_t)tp, vpa = (u32)(uintptr_t)vp;
    STAMP(2, 2);
    __builtin_amdgcn_s_setprio(3);
    while (true) {
      asm volatile(
          "s_nop 7\n\ts_nop 7\n"
          "1:\n\t"
          XSTEP(32, 48, 64, 80, 0) XSTEP(64, 80, 32, 48, 2368) XSTEP(32, 48, 64, 80, 4736) XSTEP(64, 80, 32, 48, 7104)
          "v_add_u32 v144, 0x2500, v144\n\tv_add_u32 v145, 0x2500, v145\n\tv_add_u32 v146, 0x2500, v146\n\t"
          "v_max_f32 v147, v10, v26\n\t"
          "s_add_u32 %[t], %[t], 4\n\t"
          "v_cmp_lt_f32 vcc, %[trig], v147\n\t"
          "s_cmp_ge_u32 %[t], 64\n\t"
          "s_cbranch_scc1 2f\n\t"
          "s_cbranch_vccz 1b\n"
          "2:\n\t"
          "s_waitcnt lgkmcnt(0)\n\ts_nop 7\n\ts_nop 7"
          : "+{v[0:15]}"(oA), "+{v[16:31]}"(oB), "+{v[32:47]}"(s0A), "+{v[48:63]}"(s0B), "=&{v[64:79]}"(s1A), "=&{v[80:95]}"(s1B),
            "+{v[96:99]}"(pA0), "+{v[100:103]}"(pA1), "+{v[104:107]}"(pB0), "+{v[108:111]}"(pB1),
            "+{v[128:131]}"(km), "+{v[132:135]}"(kt), "+{v[136:139]}"(v0), "+{v[140:143]}"(v1),
            "+{v144}"(kpa), "+{v145}"(tpa), "+{v146}"(vpa), [t] "+s"(t)
          : "{v[112:115]}"(qm[0]), "{v[116:119]}"(qm[1]), "{v[120:123]}"(qt[0]), "{v[124:127]}"(qt[1]), [trig] "s"(L_TRIG)
          : "v147", "vcc", "scc", "memory");
      if (t >= 64) break;
      oA = mfma32(v0, pA0, oA); oB = mfma32(v0, pB0, oB); oA = mfma32(v1, pA1, oA); oB = mfma32(v1, pB1, oB);
      pA0 = (u32x4){0u, 0u, 0u, 0u}; pA1 = pA0; pB0 = pA0; pB1 = pA0;
      const float nA = set_ref(mA + fmaxf(floorf(__builtin_amdgcn_logf(oA[10])) + M_MARGIN, 0.f), qt[0]);
      const float nB = set_ref(mB + fmaxf(floorf(__builtin_amdgcn_logf(oB[10])) + M_MARGIN, 0.f), qt[1]);
      const float dA = nA - mA, dB = nB - mB;
      const int eA = -(int)dA, eB = -(int)dB;
#pragma unroll
      for (int i = 0; i < 16; i++) { oA[i] = __builtin_amdgcn_ldexpf(oA[i], eA); oB[i] = __builtin_amdgcn_ldexpf(oB[i], eB); s0A[i] -= dA; s0B[i] -= dB; }
      mA = nA; mB = nB;
    }
    __builtin_amdgcn_s_setprio(0);
    STAMP(2, 3);
    oA = mfma32(v0, pA0, oA); oB = mfma32(v0, pB0, oB); oA = mfma32(v1, pA1, oA); oB = mfma32(v1, pB1, oB);
    {
      float tA = 0.f, tB = 0.f;
#pragma unroll
      for (int i = 0; i < 11; i++) { tA += fabsf(oA[i]); tB += fabsf(oB[i]); }
      const bool ok = tA < 3.0e38f && tB < 3.0e38f && oA[10] > 1.0e-30f && oB[10] > 1.0e-30f;
      if (__builtin_expect(!__all(ok), 0)) {
        const char* gk = ws + WS_KV;
        qt[0][1] = 0u; qt[1][1] = 0u;
        float a = -INFINITY, b = -INFINITY;
        for (int t = 0; t < 64; t++) {
          km = *(const u32x4*)(gk + t * KVT + koff); kt[0] = *(const u32*)(gk + t * KVT + toff);
          s0A = mfma32(kt, qt[0], mfma32(km, qm[0], zero)); s0B = mfma32(kt, qt[1], mfma32(km, qm[1], zero));
#pragma unroll
          for (int i = 0; i < 16; i++) { a = fmaxf(a, s0A[i]); b = fmaxf(b, s0B[i]); }
        }
        a = fmaxf(a, __shfl_xor(a, 32)); b = fmaxf(b, __shfl_xor(b, 32));
        oA = zero; oB = zero;
        for (int t = 0; t < 64; t++) {
          km = *(const u32x4*)(gk + t * KVT + koff); kt[0] = *(const u32*)(gk + t * KVT + toff);
          v0 = *(const u32x4*)(gk + t * KVT + voff); v1 = *(const u32x4*)(gk + t * KVT + voff + 608);
          s0A = mfma32(kt, qt[0], mfma32(km, qm[0], zero)); s0B = mfma32(kt, qt[1], mfma32(km, qm[1], zero));
#pragma unroll
          for (int i = 0; i < 16; i++) { s0A[i] = __builtin_amdgcn_exp2f(s0A[i] - a); s0B[i] = __builtin_amdgcn_exp2f(s0B[i] - b); }
#pragma unroll
          for (int i = 0; i < 4; i++) { pA0[i] = pk(s0A[2 * i], s0A[2 * i + 1]); pA1[i] = pk(s0A[8 + 2 * i], s0A[9 + 2 * i]); pB0[i] = pk(s0B[2 * i], s0B[2 * i + 1]); pB1[i] = pk(s0B[8 + 2 * i], s0B[9 + 2 * i]); }
          oA = mfma32(v0, pA0, oA); oB = mfma32(v0, pB0, oB); oA = mfma32(v1, pA1, oA); oB = mfma32(v1, pB1, oB);
        }
      }
    }
#pragma unroll
    for (int t = 0; t < 2; t++) {
      const f32x16& o = t == 0 ? oA : oB;
      const float inv = 1.0f / o[10];
      const int m = qb * 64 + t * 32 + r;
      char* ob = ws + WS_OF + (size_t)(m >> 4) * 18 * 1024 + (m & 15) * 16;
      const int ch = hg * 9 + wave * 2 + h, ct = hg * 9 + 8;
      u32x4 w = {pk(o[0] * inv, o[1] * inv), pk(o[2] * inv, o[3] * inv), pk(o[4] * inv, o[5] * inv), pk(o[6] * inv, o[7] * inv)};
      *(u32x4*)(ob + ch * 256) = w;
      if (h == 0) *(u32*)(ob + ct * 256 + wave * 4) = pk(o[8] * inv, o[9] * inv);
    }
    STAMP(2, 4);
    __builtin_amdgcn_s_barrier();
    __builtin_amdgcn_s_barrier();
  } else {
    const int w = wave - 4, kh = xcd >> 2, rt = (xcd & 3) * 8 + (idx & 7), cu = idx >> 3, cp = cu >> 1, u = cu & 1;
    const int k0 = kh * 144 + w * 36;
    const __amdgpu_buffer_rsrc_t rs = __builtin_amdgcn_make_buffer_rsrc(ws, 0, 0x7fffffff, 0x00020000);
    const u32 hoff0 = (u32)WS_HF + ((u32)(rt * 2 + u) * 288 + k0) * 1024, hoff1 = (u32)WS_HF + ((u32)(rt * 2 + 1 - u) * 288 + k0) * 1024;
    const u32 woff = (u32)WS_W2T + ((u32)(cp * 9 + u * 5) * 288 + k0) * 1024, woff4 = (u32)WS_W2T + ((u32)(cp * 9 + 4) * 288 + k0) * 1024;
    const u32 lo = lane * 16;
    f32x16 acc[9];
    u32x4 hf[3][2], wf[3][5];
#define BLD(off) __builtin_amdgcn_raw_buffer_load_b128(rs, lo, off, 0)
#define GLOAD(b, ks) do { hf[b][0] = BLD(hoff0 + (ks) * 1024); hf[b][1] = BLD(hoff1 + (ks) * 1024); \
      _Pragma("unroll") for (int i = 0; i < 4; i++) wf[b][i] = BLD(woff + i * 288 * 1024 + (ks) * 1024); \
      wf[b][4] = BLD(woff4 + (ks) * 1024); } while (0)
    WAITV(0);
    __builtin_amdgcn_s_barrier();
    STAMP(2, 1);
    GLOAD(0, 0); GLOAD(1, 1); GLOAD(2, 2);
#pragma unroll
    for (int i = 0; i < 9; i++)
#pragma unroll
      for (int e = 0; e < 16; e++) acc[i][e] = 0.f;
#define GSTEP(b) do { _Pragma("unroll") for (int i = 0; i < 4; i++) { acc[2 * i] = mfma32(wf[b][i], hf[b][0], acc[2 * i]); acc[2 * i + 1] = mfma32(wf[b][i], hf[b][1], acc[2 * i + 1]); } \
      acc[8] = mfma32(wf[b][4], hf[b][0], acc[8]); } while (0)
#pragma unroll 1
    for (int ks = 0; ks < 33; ks += 3) {
      GSTEP(0); SBAR(); GLOAD(0, ks + 3); SBAR(); GSTEP(1); SBAR(); GLOAD(1, ks + 4); SBAR(); GSTEP(2); SBAR(); GLOAD(2, ks + 5); SBAR();
    }
    GSTEP(0); GSTEP(1); GSTEP(2);
    STAMP(2, 3);
    __builtin_amdgcn_s_barrier();
#pragma unroll
    for (int i = 0; i < 9; i++)
#pragma unroll
      for (int o = 0; o < 4; o++)
        *(LAS f32x4*)(lds + ((o * 4 + w) * 9 + i) * 1024 + lane * 16) = (f32x4){acc[i][4 * o], acc[i][4 * o + 1], acc[i][4 * o + 2], acc[i][4 * o + 3]};
    asm volatile("s_waitcnt lgkmcnt(0)\n\ts_barrier" ::: "memory");
#pragma unroll
    for (int i = 0; i < 9; i++) {
      f32x4 v = *(LAS const f32x4*)(lds + ((w * 4) * 9 + i) * 1024 + lane * 16);
#pragma unroll
      for (int j = 1; j < 4; j++) v += *(LAS const f32x4*)(lds + ((w * 4 + j) * 9 + i) * 1024 + lane * 16);
      const int nt = i < 8 ? cp * 9 + u * 5 + (i >> 1) : cp * 9 + 4, mt = rt * 2 + ((i & 1) ? 1 - u : u);
      const int n4 = nt * 8 + 2 * w + h;
      *(u32x2*)(ws + WS_G + (((size_t)kh * 144 + n4) * 2048 + mt * 32 + r) * 8) = (u32x2){pk(v[0], v[1]), pk(v[2], v[3])};
    }
    STAMP(2, 4);
  }
}

__global__ __launch_bounds__(576) void k3_out(const u16* __restrict__ x, const u16* __restrict__ b2, const char* __restrict__ ws, u16* __restrict__ out) {
  __shared__ __attribute__((aligned(16))) char lds[36 * 1024];
  const int tid = threadIdx.x, lane = tid & 63, wave = __builtin_amdgcn_readfirstlane(tid >> 6);
  const int xcd = blockIdx.x & 7, idx = blockIdx.x >> 3;
  const int mb = xcd * 8 + (idx & 7), cg = idx >> 3;
  STAMP(3, 0);
  const u32 ldsb = (u32)(uintptr_t)(LAS char*)lds;
#pragma unroll
  for (int i = 0; i < 4; i++) dma16(ws + WS_OF + ((size_t)mb * 36 + wave * 4 + i) * 1024, lane * 16, ldsb + (wave * 4 + i) * 1024);
  const int nt = cg * 9 + wave, c = lane & 15, gq = lane >> 4;
  u32x4 wf[18];
#pragma unroll
  for (int ks = 0; ks < 18; ks++) wf[ks] = *(const u32x4*)(ws + WS_WOT + ((size_t)(nt * 18 + ks) * 64 + lane) * 16);
  const int n = nt * 16 + gq * 4;
  u32x2 g0[2], g1[2], xv[2];
#pragma unroll
  for (int t = 0; t < 2; t++) {
    const int m = mb * 32 + t * 16 + c;
    g0[t] = *(const u32x2*)(ws + WS_G + ((size_t)(n >> 2) * 2048 + m) * 8);
    g1[t] = *(const u32x2*)(ws + WS_G + ((size_t)(144 + (n >> 2)) * 2048 + m) * 8);
    xv[t] = *(const u32x2*)(x + (size_t)m * D + n);
  }
  const u32x2 bv = *(const u32x2*)(b2 + n);
  asm volatile("s_waitcnt vmcnt(25)\n\ts_barrier" ::: "memory");
  STAMP(3, 1);
  f32x4 acc[2] = {{0.f, 0.f, 0.f, 0.f}, {0.f, 0.f, 0.f, 0.f}};
#pragma unroll
  for (int ks = 0; ks < 18; ks++)
#pragma unroll
    for (int t = 0; t < 2; t++) acc[t] = mfma16(wf[ks], *(LAS const u32x4*)(lds + (t * 18 + ks) * 1024 + lane * 16), acc[t]);
#pragma unroll
  for (int t = 0; t < 2; t++) {
    float v[4];
#pragma unroll
    for (int e = 0; e < 4; e++) {
      const int sh = (e & 1) * 16;
      v[e] = acc[t][e] + bf2f((g0[t][e >> 1] >> sh) & 0xffffu) + bf2f((g1[t][e >> 1] >> sh) & 0xffffu) + bf2f((xv[t][e >> 1] >> sh) & 0xffffu) + bf2f((bv[e >> 1] >> sh) & 0xffffu);
    }
    const int m = mb * 32 + t * 16 + c;
    *(u32x2*)(out + (size_t)m * D + n) = (u32x2){pk(v[0], v[1]), pk(v[2], v[3])};
  }
  STAMP(3, 4);
}

extern "C" void kernel_launch(void* const* d_in, const int* in_sizes, int n_in, void* d_out, int out_size, void* d_ws, size_t ws_size, hipStream_t stream) {
  const u16* x = (const u16*)d_in[0]; const u16* wq = (const u16*)d_in[1]; const u16* wk = (const u16*)d_in[2]; const u16* wv = (const u16*)d_in[3];
  const u16* wo = (const u16*)d_in[4]; const u16* w1 = (const u16*)d_in[6]; const u16* b1 = (const u16*)d_in[7]; const u16* w2 = (const u16*)d_in[8];
  const u16* b2 = (const u16*)d_in[9];
  char* ws = (char*)d_ws;
  k0_prep<<<256, 256, 0, stream>>>(x, wk, wv, wo, w2, ws);
  k1_gemm<<<256, 256, 0, stream>>>(w1, wq, b1, ws);
  k2_attn_gemm<<<256, 512, 0, stream>>>(ws);
  k3_out<<<256, 576, 0, stream>>>(x, b2, ws, (u16*)d_out);
}
```

```cpp
#include <hip/hip_runtime.h>
#include <stdint.h>

typedef unsigned short u16;
typedef unsigned int u32;
typedef __bf16 bf16x8 __attribute__((ext_vector_type(8)));
typedef __bf16 bf16x2 __attribute__((ext_vector_type(2)));
typedef float f32x2 __attribute__((ext_vector_type(2)));
typedef float f32x4 __attribute__((ext_vector_type(4)));
typedef float f32x16 __attribute__((ext_vector_type(16)));
typedef u32 u32x2 __attribute__((ext_vector_type(2)));
typedef u32 u32x4 __attribute__((ext_vector_type(4)));
typedef u32 u32x8 __attribute__((ext_vector_type(8)));
typedef u32 u32x10 __attribute__((ext_vector_type(10)));
typedef u32 u32x12 __attribute__((ext_vector_type(12)));
typedef u32 u32x16 __attribute__((ext_vector_type(16)));
#define LAS __attribute__((address_space(3)))
#define GAS __attribute__((address_space(1)))
#define DI __device__ __forceinline__
#define SBAR() __builtin_amdgcn_sched_barrier(0)
#define PIN(x) asm volatile("" : "+v"(x))

constexpr int S = 2048, D = 576, F = 4608;
constexpr float QSCALE = 0.23570226039551584f * 1.4426950408889634f;

constexpr int KVG = 9728;
constexpr size_t WS_KV  = 0;
constexpr size_t WS_CNT = (size_t)160 * 1024;
constexpr size_t WS_CNG = WS_CNT + 64 * 256;
constexpr size_t WS_XF  = WS_CNG + 32 * 128;
constexpr size_t WS_OF  = WS_XF + (size_t)64 * 36 * 1024;
constexpr size_t WS_WOT = WS_OF + (size_t)64 * 36 * 1024;
constexpr size_t WS_G   = WS_WOT + (size_t)648 * 1024;
constexpr size_t WS_HF  = WS_G + (size_t)80 * 64 * 2048;

DI float bf2f(u32 v) { return __uint_as_float(v << 16); }
DI u32 pk(float a, float b) { f32x2 v = {a, b}; return __builtin_bit_cast(u32, __builtin_convertvector(v, bf16x2)); }
DI float rbf(float a) { return __uint_as_float(pk(a, 0.f) << 16); }
DI f32x16 mfma32(u32x4 a, u32x4 b, f32x16 c) { return __builtin_amdgcn_mfma_f32_32x32x16_bf16(__builtin_bit_cast(bf16x8, a), __builtin_bit_cast(bf16x8, b), c, 0, 0, 0); }
DI void dma16(const void* sbase, u32 voff, u32 ldsaddr) {
  asm volatile("s_mov_b32 m0, %2\n\ts_nop 0\n\tglobal_load_lds_dwordx4 %0, %1" :: "v"(voff), "s"(sbase), "s"(ldsaddr) : "memory", "m0");
}
DI void bdma16(u32x4 srd, u32 voff, u32 soff, u32 ldsaddr) {
  asm volatile("s_mov_b32 m0, %3\n\ts_nop 0\n\tbuffer_load_dwordx4 %0, %1, %2 offen lds" :: "v"(voff), "s"(srd), "s"(soff), "s"(ldsaddr) : "memory", "m0");
}
#define RFL(x) ((u32)__builtin_amdgcn_readfirstlane((u32)(x)))
DI u32x4 mksrd(const void* p, u32 bytes) {
  const uint64_t a = (uint64_t)p;
  return (u32x4){RFL((u32)a), RFL((u32)(a >> 32) & 0xffffu), RFL(bytes), 0x00020000u};
}
DI int lane_id() { int l; asm volatile("v_mbcnt_lo_u32_b32 %0, -1, 0\n\tv_mbcnt_hi_u32_b32 %0, -1, %0" : "=v"(l)); return l; }
DI void st16(char* base, u32 off, u32x4 v) { __builtin_amdgcn_raw_buffer_store_b128(v, __builtin_amdgcn_make_buffer_rsrc(base, 0, 0x7fffffff, 0x00020000), off, 0, 16); }
DI void st8(char* base, u32 off, u32x2 v) { __builtin_amdgcn_raw_buffer_store_b64(v, __builtin_amdgcn_make_buffer_rsrc(base, 0, 0x7fffffff, 0x00020000), off, 0, 16); }
#define WAITV(n) asm volatile("s_waitcnt vmcnt(" #n ")" ::: "memory")
#define LBAR() asm volatile("s_waitcnt lgkmcnt(0)\n\ts_barrier" ::: "memory")
#define VBAR(n) asm volatile("s_waitcnt vmcnt(" #n ")\n\ts_barrier" ::: "memory")

constexpr int K0_W = 18432, K0_R = K0_W + 6 * 8192, K0_LDS = K0_R + 6 * 3584;
#define DPP_ADD(v, ctrl) v += __builtin_bit_cast(float, __builtin_amdgcn_update_dpp(0, __builtin_bit_cast(int, v), ctrl, 0xf, 0xf, true))
DI float half_sum(float v) {
  DPP_ADD(v, 0xB1); DPP_ADD(v, 0x4E); DPP_ADD(v, 0x141); DPP_ADD(v, 0x140);
  const auto t = __builtin_amdgcn_permlane16_swap(__float_as_uint(v), __float_as_uint(v), false, false);
  return __uint_as_float(t[0]) + __uint_as_float(t[1]);
}
__global__ __launch_bounds__(896) void k0_prep(const u16* __restrict__ x, const u16* __restrict__ wk, const u16* __restrict__ wv, const u16* __restrict__ w1,
                                               const u16* __restrict__ wq, const u16* __restrict__ wo, char* __restrict__ ws) {
  __shared__ __attribute__((aligned(16))) char lds[K0_LDS];
  asm volatile("" :: "s"(x), "s"(wk), "s"(wv), "s"(w1), "s"(wq), "s"(wo), "s"(ws));
  const int lane = threadIdx.x & 63, wave = __builtin_amdgcn_readfirstlane(threadIdx.x >> 6);
  const int r = lane & 31, h = lane >> 5;
  const u32 ldsb = (u32)(uintptr_t)(LAS char*)lds;
  if (blockIdx.x >= 128) {
    const int bi = (blockIdx.x - 128) >> 3, kq = (blockIdx.x & 7) >> 1;
    if (blockIdx.x < 208 && wave == 13) ((u32*)(ws + WS_CNT))[(blockIdx.x - 128) * 64 + lane] = 0u;
    u32 total; uint64_t pc;
    asm volatile("s_getpc_b64 %0\n.Lcode_here:\n\ts_mov_b32 %1, .Lcode_end-.Lcode_here" : "=s"(pc), "=s"(total));
    const u32 rb = (bi * 2 + (blockIdx.x & 1)) * 18;
    const u32 i0 = min(max(wave - 8, 0) * 64 + lane, 323), r0 = i0 / 18, iq = min(lane, 53), rq = iq / 3, oc = (bi + 16 * lane) * 128;
    const u32 off = wave >= 8 ? (rb + r0) * (F * 2) + kq * 2304 + (i0 - 18 * r0) * 128 : wave == 7 ? (rb + rq) * (D * 2) + kq * 288 + (iq - 3 * rq) * 128 + 16 : oc < total ? oc : 0u;
    const uint64_t base = wave >= 8 ? (uint64_t)w1 : wave == 7 ? (uint64_t)wq : pc;
    if (wave >= 6) asm volatile("global_load_dword v127, %0, %1" :: "v"(off), "s"(base) : "v127", "memory");
    { const u32 t0 = (blockIdx.x - 128) * 896 + threadIdx.x;
      const u32x4 none = {~0u, ~0u, ~0u, ~0u};
      st16(ws, (u32)WS_OF + t0 * 16, none);
      if (t0 < 147456 - 114688) st16(ws, (u32)WS_OF + (114688 + t0) * 16, none); }
    const int fr = wave * 128 + (blockIdx.x - 128);
    u16 e[8];
    if (fr < 648) {
      const int ks = fr / 18, nt = fr - 18 * ks, kq2 = ks / 9, st = ks - 9 * kq2;
#pragma unroll
      for (int j = 0; j < 8; j++) { const int rho = 8 * (j >> 2) + 4 * h + (j & 3); e[j] = wo[(size_t)(144 * kq2 + (st < 8 ? 18 * st + rho : 18 * (rho >> 1) + 16 + (rho & 1))) * D + nt * 32 + r]; }
    }
    if (fr < 648) st16(ws, (u32)WS_WOT + (fr * 64 + lane) * 16, ((u32x4){e[0] | ((u32)e[1] << 16), e[2] | ((u32)e[3] << 16), e[4] | ((u32)e[5] << 16), e[6] | ((u32)e[7] << 16)}));
    return;
  }
  const int mt = blockIdx.x >> 1, ro = (blockIdx.x & 1) * 16;
  if (wave >= 6) {
    const int w = wave - 6, rl = w * 2 + h;
    const u16* xr = x + (size_t)(mt * 32 + ro + rl) * D;
    u32x4 v[3];
#pragma unroll
    for (int i = 0; i < 3; i++) v[i] = *(const u32x4*)(xr + min(r + 32 * i, 71) * 8);
    float f[24];
#pragma unroll
    for (int i = 0; i < 3; i++)
#pragma unroll
      for (int e = 0; e < 4; e++) { f[8 * i + 2 * e] = bf2f(v[i][e] & 0xffffu); f[8 * i + 2 * e + 1] = __uint_as_float(v[i][e] & 0xffff0000u); }
    float s0 = 0.f, s1 = 0.f;
#pragma unroll
    for (int i = 0; i < 16; i++) s0 += f[i];
#pragma unroll
    for (int i = 16; i < 24; i++) s1 += f[i];
    const float mean = half_sum(s0 + (r < 8 ? s1 : 0.f)) * (1.0f / D);
    s0 = 0.f; s1 = 0.f;
#pragma unroll
    for (int i = 0; i < 16; i++) s0 += (f[i] - mean) * (f[i] - mean);
#pragma unroll
    for (int i = 16; i < 24; i++) s1 += (f[i] - mean) * (f[i] - mean);
    const float var = rbf(half_sum(s0 + (r < 8 ? s1 : 0.f)) * (1.0f / D)), rs = 1.0f / rbf(sqrtf(rbf(var + 1e-5f))), mb = rbf(mean);
#pragma unroll
    for (int i = 0; i < 3; i++) {
      u32x4 o;
#pragma unroll
      for (int e = 0; e < 4; e++) {
        const u32 c = pk(f[8 * i + 2 * e] - mb, f[8 * i + 2 * e + 1] - mb);
        o[e] = pk(bf2f(c & 0xffffu) * rs, __uint_as_float(c & 0xffff0000u) * rs);
      }
      const int ch = min(r + 32 * i, 71);
      *(LAS u32x4*)(lds + ch * 256 + ((rl ^ ch) & 15) * 16) = o;
    }
    LBAR();
#pragma unroll
    for (int i = 0; i < 3; i++) {
      const int ch = min((w * 3 + i) * 4 + (lane >> 4), 71);
      st16(ws, (u32)WS_XF + (mt * 36 * 64 + ch * 32 + ro + (lane & 15)) * 16, *(LAS const u32x4*)(lds + ch * 256 + ((lane ^ ch) & 15) * 16));
    }
    LBAR();
    if (w < 5) {
      const u32 off = w < 3 ? w * 512 + (h * 16 + (r & 15)) * 16 : 1536 + (w - 3) * 1024 + lane * 16;
      f32x4 t = *(LAS const f32x4*)(lds + K0_R + off);
#pragma unroll
      for (int j = 1; j < 6; j++) t += *(LAS const f32x4*)(lds + K0_R + j * 3584 + off);
      char* kv = ws + WS_KV + (size_t)(mt >> 2) * KVG;
      const int tl = mt & 3;
      u32x2 o = {pk(t[0], t[1]), pk(t[2], t[3])};
      if (w < 2) { if (r < 16) *(u32x2*)(kv + tl * 1024 + (h * 32 + ro + r) * 16 + w * 8) = o; }
      else if (w == 2) { if (lane < 16) *(u32*)(kv + 4096 + (ro + r) * 16 + tl * 4) = o[0]; }
      else if (r < 20) {
        if (r >= 18) { const u32 c = r == 18 ? 0x3f803f80u : 0u; o = (u32x2){c, c}; }
        *(u32x2*)(kv + 4608 + tl * 1280 + (((ro >> 4) * 2 + h) * 20 + r) * 16 + (w - 3) * 8) = o;
      }
    }
  } else {
    const int w = wave;
    const u32 wb = ldsb + K0_W + w * 8192;
    { const u32x4 sk = mksrd(wk + w * 96 * 18, 3456), sv = mksrd(wv + w * 96 * 18, 3456);
      asm volatile("s_nop 4" ::: "memory");
#pragma unroll
      for (int i = 0; i < 4; i++) { bdma16(sk, lane * 16, i * 1024, wb + i * 1024); bdma16(sv, lane * 16, i * 1024, wb + 4096 + i * 1024); } }
    WAITV(0);
    u32x4 wf[2][6];
    LAS const u16* wp = (LAS const u16*)(uintptr_t)wb + h * 8 * 18 + min(r, 17);
#pragma unroll
    for (int m = 0; m < 2; m++)
#pragma unroll
      for (int i = 0; i < 6; i++)
#pragma unroll
        for (int e = 0; e < 4; e++) wf[m][i][e] = wp[m * 2048 + (i * 16 + 2 * e) * 18] | ((u32)wp[m * 2048 + (i * 16 + 2 * e + 1) * 18] << 16);
#pragma unroll
    for (int i = 0; i < 6; i++) { PIN(wf[0][i]); PIN(wf[1][i]); }
    LBAR();
    f32x16 ak, av;
#pragma unroll
    for (int i = 0; i < 16; i++) { ak[i] = 0.f; av[i] = 0.f; }
#pragma unroll
    for (int i = 0; i < 6; i++) {
      const int ch = (w * 6 + i) * 2 + h;
      const u32x4 xf = *(LAS const u32x4*)(lds + ch * 256 + ((r ^ ch) & 15) * 16);
      ak = mfma32(wf[0][i], xf, ak); av = mfma32(xf, wf[1][i], av);
    }
    LAS char* rb = (LAS char*)lds + K0_R + w * 3584;
    if (r < 16) {
#pragma unroll
      for (int i = 0; i < 3; i++) *(LAS f32x4*)(rb + i * 512 + (h * 16 + r) * 16) = (f32x4){ak[4 * i], ak[4 * i + 1], ak[4 * i + 2], ak[4 * i + 3]};
    }
    *(LAS f32x4*)(rb + 1536 + lane * 16) = (f32x4){av[0], av[1], av[2], av[3]};
    *(LAS f32x4*)(rb + 2560 + lane * 16) = (f32x4){av[4], av[5], av[6], av[7]};
    LBAR();
  }
}

constexpr int P1_STAGE = 24576;
constexpr int K2_BATCH = 2 * KVG, K2_WBASE = 4 * K2_BATCH, K2_FL = K2_WBASE + 8 * 9216, P1_BIAS = K2_FL + 2048, K1_LDS = K2_FL + KVG + 64;
constexpr float M_MARGIN = 80.f, L_TRIG = 1024.f;
#define P1_MAIN \
    "s_mov_b32 s76, 0\n\t" \
    "buffer_load_dwordx4 v[0:3], v249, s[72:75], s76 offen offset:0\n\t" \
    "buffer_load_dwordx4 v[4:7], v249, s[72:75], s76 offen offset:1024\n\t" \
    "buffer_load_dwordx4 v[8:11], v249, s[72:75], s76 offen offset:2048\n\t" \
    "buffer_load_dwordx4 v[12:15], v249, s[72:75], s76 offen offset:3072\n\t" \
    "s_mov_b32 s76, 4096\n\t" \
    "buffer_load_dwordx4 v[16:19], v249, s[72:75], s76 offen offset:0\n\t" \
    "buffer_load_dwordx4 v[20:23], v249, s[72:75], s76 offen offset:1024\n\t" \
    "buffer_load_dwordx4 v[24:27], v249, s[72:75], s76 offen offset:2048\n\t" \
    "buffer_load_dwordx4 v[28:31], v249, s[72:75], s76 offen offset:3072\n\t" \
    "s_mov_b32 s76, 8192\n\t" \
    "buffer_load_dwordx4 v[32:35], v249, s[72:75], s76 offen offset:0\n\t" \
    "buffer_load_dwordx4 v[36:39], v249, s[72:75], s76 offen offset:1024\n\t" \
    "buffer_load_dwordx4 v[40:43], v249, s[72:75], s76 offen offset:2048\n\t" \
    "buffer_load_dwordx4 v[44:47], v249, s[72:75], s76 offen offset:3072\n\t" \
    "s_mov_b32 s76, 12288\n\t" \
    "buffer_load_dwordx4 v[48:51], v249, s[72:75], s76 offen offset:0\n\t" \
    "buffer_load_dwordx4 v[52:55], v249, s[72:75], s76 offen offset:1024\n\t" \
    "buffer_load_dwordx4 v[56:59], v249, s[72:75], s76 offen offset:2048\n\t" \
    "buffer_load_dwordx4 v[60:63], v249, s[72:75], s76 offen offset:3072\n\t" \
    "s_mov_b32 s70, 3\n\t" \
    "s_barrier\n\t" \
    "ds_read_b64_tr_b16 v[208:209], v246 offset:0\n\t" \
    "ds_read_b64_tr_b16 v[210:211], v246 offset:256\n\t" \
    "ds_read_b64_tr_b16 v[212:213], v246 offset:1024\n\t" \
    "ds_read_b64_tr_b16 v[214:215], v246 offset:1280\n\t" \
    "ds_read_b64_tr_b16 v[216:217], v246 offset:2048\n\t" \
    "ds_read_b64_tr_b16 v[218:219], v246 offset:2304\n\t" \
    "ds_read_b64_tr_b16 v[220:221], v246 offset:3072\n\t" \
    "ds_read_b64_tr_b16 v[222:223], v246 offset:3328\n\t" \
    "s_waitcnt vmcnt(14)\n\t" \
    "s_waitcnt lgkmcnt(6)\n\t" \
    "v_mfma_f32_32x32x16_bf16 v[144:159], v[208:211], v[0:3], 0\n\t" \
    "ds_read_b64_tr_b16 v[224:225], v246 offset:4096\n\t" \
    "ds_read_b64_tr_b16 v[226:227], v246 offset:4352\n\t" \
    "s_mov_b32 s76, 16384\n\t" \
    "buffer_load_dwordx4 v[64:67], v249, s[72:75], s76 offen offset:0\n\t" \
    "s_waitcnt lgkmcnt(6)\n\t" \
    "v_mfma_f32_32x32x16_bf16 v[144:159], v[212:215], v[4:7], v[144:159]\n\t" \
    "ds_read_b64_tr_b16 v[228:229], v246 offset:5120\n\t" \
    "ds_read_b64_tr_b16 v[230:231], v246 offset:5376\n\t" \
    "buffer_load_dwordx4 v[68:71], v249, s[72:75], s76 offen offset:1024\n\t" \
    "s_waitcnt vmcnt(14)\n\t" \
    "s_waitcnt lgkmcnt(6)\n\t" \
    "v_mfma_f32_32x32x16_bf16 v[144:159], v[216:219], v[8:11], v[144:159]\n\t" \
    "ds_read_b64_tr_b16 v[208:209], v246 offset:6144\n\t" \
    "ds_read_b64_tr_b16 v[210:211], v246 offset:6400\n\t" \
    "buffer_load_dwordx4 v[72:75], v249, s[72:75], s76 offen offset:2048\n\t" \
    "s_waitcnt lgkmcnt(6)\n\t" \
    "v_mfma_f32_32x32x16_bf16 v[144:159], v[220:223], v[12:15], v[144:159]\n\t" \
    "ds_read_b64_tr_b16 v[212:213], v246 offset:7168\n\t" \
    "ds_read_b64_tr_b16 v[214:215], v246 offset:7424\n\t" \
    "buffer_load_dwordx4 v[76:79], v249, s[72:75], s76 offen offset:3072\n\t" \
    "s_waitcnt vmcnt(14)\n\t" \
    "s_waitcnt lgkmcnt(6)\n\t" \
    "v_mfma_f32_32x32x16_bf16 v[144:159], v[224:227], v[16:19], v[144:159]\n\t" \
    "ds_read_b64_tr_b16 v[216:217], v246 offset:8192\n\t" \
    "ds_read_b64_tr_b16 v[218:219], v246 offset:8448\n\t" \
    "s_mov_b32 s76, 20480\n\t" \
    "buffer_load_dwordx4 v[80:83], v249, s[72:75], s76 offen offset:0\n\t" \
    "s_waitcnt lgkmcnt(6)\n\t" \
    "v_mfma_f32_32x32x16_bf16 v[144:159], v[228:231], v[20:23], v[144:159]\n\t" \
    "ds_read_b64_tr_b16 v[220:221], v246 offset:9216\n\t" \
    "ds_read_b64_tr_b16 v[222:223], v246 offset:9472\n\t" \
    "buffer_load_dwordx4 v[84:87], v249, s[72:75], s76 offen offset:1024\n\t" \
    "s_waitcnt vmcnt(14)\n\t" \
    "s_waitcnt lgkmcnt(6)\n\t" \
    "v_mfma_f32_32x32x16_bf16 v[144:159], v[208:211], v[24:27], v[144:159]\n\t" \
    "s_barrier\n\t" \
    "ds_read_b64_tr_b16 v[224:225], v246 offset:10240\n\t" \
    "ds_read_b64_tr_b16 v[226:227], v246 offset:10496\n\t" \
    "buffer_load_dwordx4 v[88:91], v249, s[72:75], s76 offen offset:2048\n\t" \
    "s_waitcnt lgkmcnt(6)\n\t" \
    "v_mfma_f32_32x32x16_bf16 v[144:159], v[212:215], v[28:31], v[144:159]\n\t" \
    "ds_read_b64_tr_b16 v[228:229], v246 offset:11264\n\t" \
    "ds_read_b64_tr_b16 v[230:231], v246 offset:11520\n\t" \
    "buffer_load_dwordx4 v[92:95], v249, s[72:75], s76 offen offset:3072\n\t" \
    "s_waitcnt vmcnt(14)\n\t" \
    "s_waitcnt lgkmcnt(6)\n\t" \
    "v_mfma_f32_32x32x16_bf16 v[144:159], v[216:219], v[32:35], v[144:159]\n\t" \
    "ds_read_b64_tr_b16 v[208:209], v246 offset:24576\n\t" \
    "ds_read_b64_tr_b16 v[210:211], v246 offset:24832\n\t" \
    "s_mov_b32 s76, 24576\n\t" \
    "buffer_load_dwordx4 v[96:99], v249, s[72:75], s76 offen offset:0\n\t" \
    "s_waitcnt lgkmcnt(6)\n\t" \
    "v_mfma_f32_32x32x16_bf16 v[144:159], v[220:223], v[36:39], v[144:159]\n\t" \
    "ds_read_b64_tr_b16 v[212:213], v246 offset:25600\n\t" \
    "ds_read_b64_tr_b16 v[214:215], v246 offset:25856\n\t" \
    "buffer_load_dwordx4 v[100:103], v249, s[72:75], s76 offen offset:1024\n\t" \
    "s_waitcnt vmcnt(14)\n\t" \
    "s_waitcnt lgkmcnt(6)\n\t" \
    "v_mfma_f32_32x32x16_bf16 v[144:159], v[224:227], v[40:43], v[144:159]\n\t" \
    "ds_read_b64_tr_b16 v[216:217], v246 offset:26624\n\t" \
    "ds_read_b64_tr_b16 v[218:219], v246 offset:26880\n\t" \
    "buffer_load_dwordx4 v[104:107], v249, s[72:75], s76 offen offset:2048\n\t" \
    "s_waitcnt lgkmcnt(6)\n\t" \
    "v_mfma_f32_32x32x16_bf16 v[144:159], v[228:231], v[44:47], v[144:159]\n\t" \
    "ds_read_b64_tr_b16 v[220:221], v246 offset:27648\n\t" \
    "ds_read_b64_tr_b16 v[222:223], v246 offset:27904\n\t" \
    "buffer_load_dwordx4 v[108:111], v249, s[72:75], s76 offen offset:3072\n\t" \
    "s_waitcnt vmcnt(14)\n\t" \
    "s_waitcnt lgkmcnt(6)\n\t" \
    "v_mfma_f32_32x32x16_bf16 v[144:159], v[208:211], v[48:51], v[144:159]\n\t" \
    "ds_read_b64_tr_b16 v[224:225], v246 offset:28672\n\t" \
    "ds_read_b64_tr_b16 v[226:227], v246 offset:28928\n\t" \
    "s_mov_b32 s76, 28672\n\t" \
    "buffer_load_dwordx4 v[112:115], v249, s[72:75], s76 offen offset:0\n\t" \
    "s_waitcnt lgkmcnt(6)\n\t" \
    "v_mfma_f32_32x32x16_bf16 v[144:159], v[212:215], v[52:55], v[144:159]\n\t" \
    "ds_read_b64_tr_b16 v[228:229], v246 offset:29696\n\t" \
    "ds_read_b64_tr_b16 v[230:231], v246 offset:29952\n\t" \
    "buffer_load_dwordx4 v[116:119], v249, s[72:75], s76 offen offset:1024\n\t" \
    "s_waitcnt vmcnt(14)\n\t" \
    "s_waitcnt lgkmcnt(6)\n\t" \
    "v_mfma_f32_32x32x16_bf16 v[144:159], v[216:219], v[56:59], v[144:159]\n\t" \
    "ds_read_b64_tr_b16 v[208:209], v246 offset:30720\n\t" \
    "ds_read_b64_tr_b16 v[210:211], v246 offset:30976\n\t" \
    "buffer_load_dwordx4 v[120:123], v249, s[72:75], s76 offen offset:2048\n\t" \
    "s_waitcnt lgkmcnt(6)\n\t" \
    "v_mfma_f32_32x32x16_bf16 v[144:159], v[220:223], v[60:63], v[144:159]\n\t" \
    "ds_read_b64_tr_b16 v[212:213], v246 offset:31744\n\t" \
    "ds_read_b64_tr_b16 v[214:215], v246 offset:32000\n\t" \
    "buffer_load_dwordx4 v[124:127], v249, s[72:75], s76 offen offset:3072\n\t" \
    "s_waitcnt vmcnt(14)\n\t" \
    "s_waitcnt lgkmcnt(6)\n\t" \
    "v_mfma_f32_32x32x16_bf16 v[144:159], v[224:227], v[64:67], v[144:159]\n\t" \
    "ds_read_b64_tr_b16 v[216:217], v246 offset:32768\n\t" \
    "ds_read_b64_tr_b16 v[218:219], v246 offset:33024\n\t" \
    "s_mov_b32 s76, 32768\n\t" \
    "buffer_load_dwordx4 v[128:131], v249, s[72:75], s76 offen offset:0\n\t" \
    "s_waitcnt lgkmcnt(6)\n\t" \
    "v_mfma_f32_32x32x16_bf16 v[144:159], v[228:231], v[68:71], v[144:159]\n\t" \
    "ds_read_b64_tr_b16 v[220:221], v246 offset:33792\n\t" \
    "ds_read_b64_tr_b16 v[222:223], v246 offset:34048\n\t" \
    "buffer_load_dwordx4 v[132:135], v249, s[72:75], s76 offen offset:1024\n\t" \
    "s_waitcnt vmcnt(14)\n\t" \
    "s_waitcnt lgkmcnt(6)\n\t" \
    "v_mfma_f32_32x32x16_bf16 v[144:159], v[208:211], v[72:75], v[144:159]\n\t" \
    "s_barrier\n\t" \
    "ds_read_b64_tr_b16 v[224:225], v246 offset:34816\n\t" \
    "ds_read_b64_tr_b16 v[226:227], v246 offset:35072\n\t" \
    "buffer_load_dwordx4 v[136:139], v249, s[72:75], s76 offen offset:2048\n\t" \
    "s_waitcnt lgkmcnt(6)\n\t" \
    "v_mfma_f32_32x32x16_bf16 v[144:159], v[212:215], v[76:79], v[144:159]\n\t" \
    "ds_read_b64_tr_b16 v[228:229], v246 offset:35840\n\t" \
    "ds_read_b64_tr_b16 v[230:231], v246 offset:36096\n\t" \
    "buffer_load_dwordx4 v[140:143], v249, s[72:75], s76 offen offset:3072\n\t" \
    "s_waitcnt vmcnt(14)\n\t" \
    "s_waitcnt lgkmcnt(6)\n\t" \
    "v_mfma_f32_32x32x16_bf16 v[144:159], v[216:219], v[80:83], v[144:159]\n\t" \
    "v_add_u32 v247, 0xc000, v247\n\t" \
    "ds_read_b64_tr_b16 v[208:209], v246 offset:49152\n\t" \
    "ds_read_b64_tr_b16 v[210:211], v246 offset:49408\n\t" \
    "s_waitcnt lgkmcnt(6)\n\t" \
    "v_mfma_f32_32x32x16_bf16 v[144:159], v[220:223], v[84:87], v[144:159]\n\t" \
    "ds_read_b64_tr_b16 v[212:213], v246 offset:50176\n\t" \
    "ds_read_b64_tr_b16 v[214:215], v246 offset:50432\n\t" \
    "s_waitcnt vmcnt(12)\n\t" \
    "s_waitcnt lgkmcnt(6)\n\t" \
    "v_mfma_f32_32x32x16_bf16 v[144:159], v[224:227], v[88:91], v[144:159]\n\t" \
    "ds_read_b64_tr_b16 v[216:217], v246 offset:51200\n\t" \
    "ds_read_b64_tr_b16 v[218:219], v246 offset:51456\n\t" \
    "s_waitcnt lgkmcnt(6)\n\t" \
    "v_mfma_f32_32x32x16_bf16 v[144:159], v[228:231], v[92:95], v[144:159]\n\t" \
    "ds_read_b64_tr_b16 v[220:221], v246 offset:52224\n\t" \
    "ds_read_b64_tr_b16 v[222:223], v246 offset:52480\n\t" \
    "s_waitcnt vmcnt(10)\n\t" \
    "s_waitcnt lgkmcnt(6)\n\t" \
    "v_mfma_f32_32x32x16_bf16 v[144:159], v[208:211], v[96:99], v[144:159]\n\t" \
    "ds_read_b64_tr_b16 v[224:225], v246 offset:53248\n\t" \
    "ds_read_b64_tr_b16 v[226:227], v246 offset:53504\n\t" \
    "s_waitcnt lgkmcnt(6)\n\t" \
    "v_mfma_f32_32x32x16_bf16 v[144:159], v[212:215], v[100:103], v[144:159]\n\t" \
    "ds_read_b64_tr_b16 v[228:229], v246 offset:54272\n\t" \
    "ds_read_b64_tr_b16 v[230:231], v246 offset:54528\n\t" \
    "s_waitcnt vmcnt(8)\n\t" \
    "s_waitcnt lgkmcnt(6)\n\t" \
    "v_mfma_f32_32x32x16_bf16 v[144:159], v[216:219], v[104:107], v[144:159]\n\t" \
    "ds_read_b64_tr_b16 v[208:209], v246 offset:55296\n\t" \
    "ds_read_b64_tr_b16 v[210:211], v246 offset:55552\n\t" \
    "s_waitcnt lgkmcnt(6)\n\t" \
    "v_mfma_f32_32x32x16_bf16 v[144:159], v[220:223], v[108:111], v[144:159]\n\t" \
    "ds_read_b64_tr_b16 v[212:213], v246 offset:56320\n\t" \
    "ds_read_b64_tr_b16 v[214:215], v246 offset:56576\n\t" \
    "s_waitcnt vmcnt(6)\n\t" \
    "s_waitcnt lgkmcnt(6)\n\t" \
    "v_mfma_f32_32x32x16_bf16 v[144:159], v[224:227], v[112:115], v[144:159]\n\t" \
    "ds_read_b64_tr_b16 v[216:217], v246 offset:57344\n\t" \
    "ds_read_b64_tr_b16 v[218:219], v246 offset:57600\n\t" \
    "s_waitcnt lgkmcnt(6)\n\t" \
    "v_mfma_f32_32x32x16_bf16 v[144:159], v[228:231], v[116:119], v[144:159]\n\t" \
    "ds_read_b64_tr_b16 v[220:221], v246 offset:58368\n\t" \
    "ds_read_b64_tr_b16 v[222:223], v246 offset:58624\n\t" \
    "s_waitcnt vmcnt(4)\n\t" \
    "s_waitcnt lgkmcnt(6)\n\t" \
    "v_mfma_f32_32x32x16_bf16 v[144:159], v[208:211], v[120:123], v[144:159]\n\t" \
    "s_barrier\n\t" \
    "ds_read_b64_tr_b16 v[224:225], v246 offset:59392\n\t" \
    "ds_read_b64_tr_b16 v[226:227], v246 offset:59648\n\t" \
    "s_waitcnt lgkmcnt(6)\n\t" \
    "v_mfma_f32_32x32x16_bf16 v[144:159], v[212:215], v[124:127], v[144:159]\n\t" \
    "ds_read_b64_tr_b16 v[228:229], v246 offset:60416\n\t" \
    "ds_read_b64_tr_b16 v[230:231], v246 offset:60672\n\t" \
    "s_waitcnt vmcnt(2)\n\t" \
    "s_waitcnt lgkmcnt(6)\n\t" \
    "v_mfma_f32_32x32x16_bf16 v[144:159], v[216:219], v[128:131], v[144:159]\n\t" \
    "s_waitcnt lgkmcnt(4)\n\t" \
    "v_mfma_f32_32x32x16_bf16 v[144:159], v[220:223], v[132:135], v[144:159]\n\t" \
    "s_waitcnt vmcnt(0)\n\t" \
    "s_waitcnt lgkmcnt(2)\n\t" \
    "v_mfma_f32_32x32x16_bf16 v[144:159], v[224:227], v[136:139], v[144:159]\n\t" \
    "ds_read_b64_tr_b16 v[208:209], v247 offset:24576\n\t" \
    "ds_read_b64_tr_b16 v[210:211], v247 offset:24832\n\t" \
    "ds_read_b64_tr_b16 v[212:213], v247 offset:25088\n\t" \
    "ds_read_b64_tr_b16 v[214:215], v247 offset:25344\n\t" \
    "s_waitcnt lgkmcnt(4)\n\t" \
    "v_mfma_f32_32x32x16_bf16 v[144:159], v[228:231], v[140:143], v[144:159]\n\t" \
    "ds_read_b64_tr_b16 v[216:217], v247 offset:26624\n\t" \
    "ds_read_b64_tr_b16 v[218:219], v247 offset:26880\n\t" \
    "ds_read_b64_tr_b16 v[220:221], v247 offset:27136\n\t" \
    "ds_read_b64_tr_b16 v[222:223], v247 offset:27392\n\t" \
    ".Lp1loop_%=:\n\t" \
    "s_waitcnt lgkmcnt(4)\n\t" \
    "v_mfma_f32_32x32x16_bf16 v[176:191], v[208:211], v[0:3], 0\n\t" \
    "ds_read_b64_tr_b16 v[224:225], v247 offset:28672\n\t" \
    "ds_read_b64_tr_b16 v[226:227], v247 offset:28928\n\t" \
    "ds_read_b128 v[232:235], v248 offset:0\n\t" \
    "v_mfma_f32_32x32x16_bf16 v[192:207], v[212:215], v[0:3], 0\n\t" \
    "ds_read_b64_tr_b16 v[228:229], v247 offset:29184\n\t" \
    "ds_read_b64_tr_b16 v[230:231], v247 offset:29440\n\t" \
    "s_waitcnt lgkmcnt(5)\n\t" \
    "v_mfma_f32_32x32x16_bf16 v[176:191], v[216:219], v[4:7], v[176:191]\n\t" \
    "ds_read_b64_tr_b16 v[208:209], v247 offset:30720\n\t" \
    "ds_read_b64_tr_b16 v[210:211], v247 offset:30976\n\t" \
    "ds_read_b128 v[240:243], v248 offset:32\n\t" \
    "v_mfma_f32_32x32x16_bf16 v[192:207], v[220:223], v[4:7], v[192:207]\n\t" \
    "ds_read_b64_tr_b16 v[212:213], v247 offset:31232\n\t" \
    "ds_read_b64_tr_b16 v[214:215], v247 offset:31488\n\t" \
    "s_waitcnt lgkmcnt(5)\n\t" \
    "v_mfma_f32_32x32x16_bf16 v[176:191], v[224:227], v[8:11], v[176:191]\n\t" \
    "ds_read_b64_tr_b16 v[216:217], v247 offset:32768\n\t" \
    "ds_read_b64_tr_b16 v[218:219], v247 offset:33024\n\t" \
    "v_mfma_f32_32x32x16_bf16 v[192:207], v[228:231], v[8:11], v[192:207]\n\t" \
    "ds_read_b64_tr_b16 v[220:221], v247 offset:33280\n\t" \
    "ds_read_b64_tr_b16 v[222:223], v247 offset:33536\n\t" \
    "s_waitcnt lgkmcnt(4)\n\t" \
    "v_mfma_f32_32x32x16_bf16 v[176:191], v[208:211], v[12:15], v[176:191]\n\t" \
    "ds_read_b64_tr_b16 v[224:225], v247 offset:34816\n\t" \
    "ds_read_b64_tr_b16 v[226:227], v247 offset:35072\n\t" \
    "v_add_f32 v244, v144, v232\n\t" \
    "v_add_f32 v245, v145, v233\n\t" \
    "v_max_f32 v244, 0, v244\n\t" \
    "v_mfma_f32_32x32x16_bf16 v[192:207], v[212:215], v[12:15], v[192:207]\n\t" \
    "ds_read_b64_tr_b16 v[228:229], v247 offset:35328\n\t" \
    "ds_read_b64_tr_b16 v[230:231], v247 offset:35584\n\t" \
    "v_max_f32 v245, 0, v245\n\t" \
    "v_mul_f32 v244, v244, v244\n\t" \
    "v_mul_f32 v245, v245, v245\n\t" \
    "v_cvt_pk_bf16_f32 v236, v244, v245\n\t" \
    "s_waitcnt lgkmcnt(4)\n\t" \
    "v_mfma_f32_32x32x16_bf16 v[176:191], v[216:219], v[16:19], v[176:191]\n\t" \
    "ds_read_b64_tr_b16 v[208:209], v247 offset:36864\n\t" \
    "ds_read_b64_tr_b16 v[210:211], v247 offset:37120\n\t" \
    "v_add_f32 v244, v146, v234\n\t" \
    "v_add_f32 v245, v147, v235\n\t" \
    "ds_read_b128 v[232:235], v248 offset:64\n\t" \
    "v_max_f32 v244, 0, v244\n\t" \
    "v_mfma_f32_32x32x16_bf16 v[192:207], v[220:223], v[16:19], v[192:207]\n\t" \
    "ds_read_b64_tr_b16 v[212:213], v247 offset:37376\n\t" \
    "ds_read_b64_tr_b16 v[214:215], v247 offset:37632\n\t" \
    "v_max_f32 v245, 0, v245\n\t" \
    "v_mul_f32 v244, v244, v244\n\t" \
    "v_mul_f32 v245, v245, v245\n\t" \
    "v_cvt_pk_bf16_f32 v237, v244, v245\n\t" \
    "s_waitcnt lgkmcnt(5)\n\t" \
    "v_mfma_f32_32x32x16_bf16 v[176:191], v[224:227], v[20:23], v[176:191]\n\t" \
    "ds_read_b64_tr_b16 v[216:217], v247 offset:38912\n\t" \
    "ds_read_b64_tr_b16 v[218:219], v247 offset:39168\n\t" \
    "v_add_f32 v244, v148, v240\n\t" \
    "v_add_f32 v245, v149, v241\n\t" \
    "v_max_f32 v244, 0, v244\n\t" \
    "v_mfma_f32_32x32x16_bf16 v[192:207], v[228:231], v[20:23], v[192:207]\n\t" \
    "ds_read_b64_tr_b16 v[220:221], v247 offset:39424\n\t" \
    "ds_read_b64_tr_b16 v[222:223], v247 offset:39680\n\t" \
    "v_max_f32 v245, 0, v245\n\t" \
    "v_mul_f32 v244, v244, v244\n\t" \
    "v_mul_f32 v245, v245, v245\n\t" \
    "v_cvt_pk_bf16_f32 v238, v244, v245\n\t" \
    "s_waitcnt lgkmcnt(4)\n\t" \
    "v_mfma_f32_32x32x16_bf16 v[176:191], v[208:211], v[24:27], v[176:191]\n\t" \
    "s_barrier\n\t" \
    "ds_read_b64_tr_b16 v[224:225], v247 offset:40960\n\t" \
    "ds_read_b64_tr_b16 v[226:227], v247 offset:41216\n\t" \
    "v_add_f32 v244, v150, v242\n\t" \
    "v_add_f32 v245, v151, v243\n\t" \
    "ds_read_b128 v[240:243], v248 offset:96\n\t" \
    "v_max_f32 v244, 0, v244\n\t" \
    "v_mfma_f32_32x32x16_bf16 v[192:207], v[212:215], v[24:27], v[192:207]\n\t" \
    "ds_read_b64_tr_b16 v[228:229], v247 offset:41472\n\t" \
    "ds_read_b64_tr_b16 v[230:231], v247 offset:41728\n\t" \
    "v_max_f32 v245, 0, v245\n\t" \
    "v_mul_f32 v244, v244, v244\n\t" \
    "v_mul_f32 v245, v245, v245\n\t" \
    "v_cvt_pk_bf16_f32 v239, v244, v245\n\t" \
    "buffer_store_dwordx4 v[236:239], v249, s[64:67], s68 offen offset:0 sc1\n\t" \
    "s_waitcnt lgkmcnt(5)\n\t" \
    "v_mfma_f32_32x32x16_bf16 v[176:191], v[216:219], v[28:31], v[176:191]\n\t" \
    "ds_read_b64_tr_b16 v[208:209], v247 offset:43008\n\t" \
    "ds_read_b64_tr_b16 v[210:211], v247 offset:43264\n\t" \
    "v_add_f32 v244, v152, v232\n\t" \
    "v_add_f32 v245, v153, v233\n\t" \
    "v_max_f32 v244, 0, v244\n\t" \
    "v_mfma_f32_32x32x16_bf16 v[192:207], v[220:223], v[28:31], v[192:207]\n\t" \
    "ds_read_b64_tr_b16 v[212:213], v247 offset:43520\n\t" \
    "ds_read_b64_tr_b16 v[214:215], v247 offset:43776\n\t" \
    "v_max_f32 v245, 0, v245\n\t" \
    "v_mul_f32 v244, v244, v244\n\t" \
    "v_mul_f32 v245, v245, v245\n\t" \
    "v_cvt_pk_bf16_f32 v236, v244, v245\n\t" \
    "s_waitcnt lgkmcnt(4)\n\t" \
    "v_mfma_f32_32x32x16_bf16 v[176:191], v[224:227], v[32:35], v[176:191]\n\t" \
    "ds_read_b64_tr_b16 v[216:217], v247 offset:45056\n\t" \
    "ds_read_b64_tr_b16 v[218:219], v247 offset:45312\n\t" \
    "v_add_f32 v244, v154, v234\n\t" \
    "v_add_f32 v245, v155, v235\n\t" \
    "ds_read_b128 v[232:235], v248 offset:128\n\t" \
    "v_max_f32 v244, 0, v244\n\t" \
    "v_mfma_f32_32x32x16_bf16 v[192:207], v[228:231], v[32:35], v[192:207]\n\t" \
    "ds_read_b64_tr_b16 v[220:221], v247 offset:45568\n\t" \
    "ds_read_b64_tr_b16 v[222:223], v247 offset:45824\n\t" \
    "v_max_f32 v245, 0, v245\n\t" \
    "v_mul_f32 v244, v244, v244\n\t" \
    "v_mul_f32 v245, v245, v245\n\t" \
    "v_cvt_pk_bf16_f32 v237, v244, v245\n\t" \
    "s_waitcnt lgkmcnt(5)\n\t" \
    "v_mfma_f32_32x32x16_bf16 v[176:191], v[208:211], v[36:39], v[176:191]\n\t" \
    "ds_read_b64_tr_b16 v[224:225], v247 offset:47104\n\t" \
    "ds_read_b64_tr_b16 v[226:227], v247 offset:47360\n\t" \
    "v_add_f32 v244, v156, v240\n\t" \
    "v_add_f32 v245, v157, v241\n\t" \
    "v_max_f32 v244, 0, v244\n\t" \
    "v_mfma_f32_32x32x16_bf16 v[192:207], v[212:215], v[36:39], v[192:207]\n\t" \
    "ds_read_b64_tr_b16 v[228:229], v247 offset:47616\n\t" \
    "ds_read_b64_tr_b16 v[230:231], v247 offset:47872\n\t" \
    "v_max_f32 v245, 0, v245\n\t" \
    "v_mul_f32 v244, v244, v244\n\t" \
    "v_mul_f32 v245, v245, v245\n\t" \
    "v_cvt_pk_bf16_f32 v238, v244, v245\n\t" \
    "s_waitcnt lgkmcnt(4)\n\t" \
    "v_mfma_f32_32x32x16_bf16 v[176:191], v[216:219], v[40:43], v[176:191]\n\t" \
    "v_add_u32 v247, 0xc000, v247\n\t" \
    "ds_read_b64_tr_b16 v[208:209], v247 offset:0\n\t" \
    "ds_read_b64_tr_b16 v[210:211], v247 offset:256\n\t" \
    "v_add_f32 v244, v158, v242\n\t" \
    "v_add_f32 v245, v159, v243\n\t" \
    "ds_read_b128 v[240:243], v248 offset:160\n\t" \
    "v_max_f32 v244, 0, v244\n\t" \
    "v_mfma_f32_32x32x16_bf16 v[192:207], v[220:223], v[40:43], v[192:207]\n\t" \
    "ds_read_b64_tr_b16 v[212:213], v247 offset:512\n\t" \
    "ds_read_b64_tr_b16 v[214:215], v247 offset:768\n\t" \
    "v_max_f32 v245, 0, v245\n\t" \
    "v_mul_f32 v244, v244, v244\n\t" \
    "v_mul_f32 v245, v245, v245\n\t" \
    "v_cvt_pk_bf16_f32 v239, v244, v245\n\t" \
    "buffer_store_dwordx4 v[236:239], v249, s[64:67], s68 offen offset:1024 sc1\n\t" \
    "s_waitcnt lgkmcnt(5)\n\t" \
    "v_mfma_f32_32x32x16_bf16 v[176:191], v[224:227], v[44:47], v[176:191]\n\t" \
    "ds_read_b64_tr_b16 v[216:217], v247 offset:2048\n\t" \
    "ds_read_b64_tr_b16 v[218:219], v247 offset:2304\n\t" \
    "v_add_f32 v244, v160, v232\n\t" \
    "v_add_f32 v245, v161, v233\n\t" \
    "v_max_f32 v244, 0, v244\n\t" \
    "v_mfma_f32_32x32x16_bf16 v[192:207], v[228:231], v[44:47], v[192:207]\n\t" \
    "ds_read_b64_tr_b16 v[220:221], v247 offset:2560\n\t" \
    "ds_read_b64_tr_b16 v[222:223], v247 offset:2816\n\t" \
    "v_max_f32 v245, 0, v245\n\t" \
    "v_mul_f32 v244, v244, v244\n\t" \
    "v_mul_f32 v245, v245, v245\n\t" \
    "v_cvt_pk_bf16_f32 v236, v244, v245\n\t" \
    "s_waitcnt lgkmcnt(4)\n\t" \
    "v_mfma_f32_32x32x16_bf16 v[176:191], v[208:211], v[48:51], v[176:191]\n\t" \
    "ds_read_b64_tr_b16 v[224:225], v247 offset:4096\n\t" \
    "ds_read_b64_tr_b16 v[226:227], v247 offset:4352\n\t" \
    "v_add_f32 v244, v162, v234\n\t" \
    "v_add_f32 v245, v163, v235\n\t" \
    "ds_read_b128 v[232:235], v248 offset:192\n\t" \
    "v_max_f32 v244, 0, v244\n\t" \
    "v_mfma_f32_32x32x16_bf16 v[192:207], v[212:215], v[48:51], v[192:207]\n\t" \
    "ds_read_b64_tr_b16 v[228:229], v247 offset:4608\n\t" \
    "ds_read_b64_tr_b16 v[230:231], v247 offset:4864\n\t" \
    "v_max_f32 v245, 0, v245\n\t" \
    "v_mul_f32 v244, v244, v244\n\t" \
    "v_mul_f32 v245, v245, v245\n\t" \
    "v_cvt_pk_bf16_f32 v237, v244, v245\n\t" \
    "s_waitcnt lgkmcnt(5)\n\t" \
    "v_mfma_f32_32x32x16_bf16 v[176:191], v[216:219], v[52:55], v[176:191]\n\t" \
    "ds_read_b64_tr_b16 v[208:209], v247 offset:6144\n\t" \
    "ds_read_b64_tr_b16 v[210:211], v247 offset:6400\n\t" \
    "v_add_f32 v244, v164, v240\n\t" \
    "v_add_f32 v245, v165, v241\n\t" \
    "v_max_f32 v244, 0, v244\n\t" \
    "v_mfma_f32_32x32x16_bf16 v[192:207], v[220:223], v[52:55], v[192:207]\n\t" \
    "ds_read_b64_tr_b16 v[212:213], v247 offset:6656\n\t" \
    "ds_read_b64_tr_b16 v[214:215], v247 offset:6912\n\t" \
    "v_max_f32 v245, 0, v245\n\t" \
    "v_mul_f32 v244, v244, v244\n\t" \
    "v_mul_f32 v245, v245, v245\n\t" \
    "v_cvt_pk_bf16_f32 v238, v244, v245\n\t" \
    "s_waitcnt lgkmcnt(4)\n\t" \
    "v_mfma_f32_32x32x16_bf16 v[176:191], v[224:227], v[56:59], v[176:191]\n\t" \
    "ds_read_b64_tr_b16 v[216:217], v247 offset:8192\n\t" \
    "ds_read_b64_tr_b16 v[218:219], v247 offset:8448\n\t" \
    "v_add_f32 v244, v166, v242\n\t" \
    "v_add_f32 v245, v167, v243\n\t" \
    "ds_read_b128 v[240:243], v248 offset:224\n\t" \
    "v_max_f32 v244, 0, v244\n\t" \
    "v_mfma_f32_32x32x16_bf16 v[192:207], v[228:231], v[56:59], v[192:207]\n\t" \
    "ds_read_b64_tr_b16 v[220:221], v247 offset:8704\n\t" \
    "ds_read_b64_tr_b16 v[222:223], v247 offset:8960\n\t" \
    "v_max_f32 v245, 0, v245\n\t" \
    "v_mul_f32 v244, v244, v244\n\t" \
    "v_mul_f32 v245, v245, v245\n\t" \
    "v_cvt_pk_bf16_f32 v239, v244, v245\n\t" \
    "buffer_store_dwordx4 v[236:239], v249, s[64:67], s68 offen offset:2048 sc1\n\t" \
    "s_waitcnt lgkmcnt(5)\n\t" \
    "v_mfma_f32_32x32x16_bf16 v[176:191], v[208:211], v[60:63], v[176:191]\n\t" \
    "ds_read_b64_tr_b16 v[224:225], v247 offset:10240\n\t" \
    "ds_read_b64_tr_b16 v[226:227], v247 offset:10496\n\t" \
    "v_add_f32 v244, v168, v232\n\t" \
    "v_add_f32 v245, v169, v233\n\t" \
    "v_max_f32 v244, 0, v244\n\t" \
    "v_mfma_f32_32x32x16_bf16 v[192:207], v[212:215], v[60:63], v[192:207]\n\t" \
    "ds_read_b64_tr_b16 v[228:229], v247 offset:10752\n\t" \
    "ds_read_b64_tr_b16 v[230:231], v247 offset:11008\n\t" \
    "v_max_f32 v245, 0, v245\n\t" \
    "v_mul_f32 v244, v244, v244\n\t" \
    "v_mul_f32 v245, v245, v245\n\t" \
    "v_cvt_pk_bf16_f32 v236, v244, v245\n\t" \
    "s_waitcnt lgkmcnt(4)\n\t" \
    "v_mfma_f32_32x32x16_bf16 v[176:191], v[216:219], v[64:67], v[176:191]\n\t" \
    "ds_read_b64_tr_b16 v[208:209], v247 offset:12288\n\t" \
    "ds_read_b64_tr_b16 v[210:211], v247 offset:12544\n\t" \
    "v_add_f32 v244, v170, v234\n\t" \
    "v_add_f32 v245, v171, v235\n\t" \
    "v_max_f32 v244, 0, v244\n\t" \
    "v_mfma_f32_32x32x16_bf16 v[192:207], v[220:223], v[64:67], v[192:207]\n\t" \
    "ds_read_b64_tr_b16 v[212:213], v247 offset:12800\n\t" \
    "ds_read_b64_tr_b16 v[214:215], v247 offset:13056\n\t" \
    "v_max_f32 v245, 0, v245\n\t" \
    "v_mul_f32 v244, v244, v244\n\t" \
    "v_mul_f32 v245, v245, v245\n\t" \
    "v_cvt_pk_bf16_f32 v237, v244, v245\n\t" \
    "s_waitcnt lgkmcnt(4)\n\t" \
    "v_mfma_f32_32x32x16_bf16 v[176:191], v[224:227], v[68:71], v[176:191]\n\t" \
    "ds_read_b64_tr_b16 v[216:217], v247 offset:14336\n\t" \
    "ds_read_b64_tr_b16 v[218:219], v247 offset:14592\n\t" \
    "v_add_f32 v244, v172, v240\n\t" \
    "v_add_f32 v245, v173, v241\n\t" \
    "v_max_f32 v244, 0, v244\n\t" \
    "v_mfma_f32_32x32x16_bf16 v[192:207], v[228:231], v[68:71], v[192:207]\n\t" \
    "ds_read_b64_tr_b16 v[220:221], v247 offset:14848\n\t" \
    "ds_read_b64_tr_b16 v[222:223], v247 offset:15104\n\t" \
    "v_max_f32 v245, 0, v245\n\t" \
    "v_mul_f32 v244, v244, v244\n\t" \
    "v_mul_f32 v245, v245, v245\n\t" \
    "v_cvt_pk_bf16_f32 v238, v244, v245\n\t" \
    "s_waitcnt lgkmcnt(4)\n\t" \
    "v_mfma_f32_32x32x16_bf16 v[176:191], v[208:211], v[72:75], v[176:191]\n\t" \
    "s_barrier\n\t" \
    "ds_read_b64_tr_b16 v[224:225], v247 offset:16384\n\t" \
    "ds_read_b64_tr_b16 v[226:227], v247 offset:16640\n\t" \
    "v_add_f32 v244, v174, v242\n\t" \
    "v_add_f32 v245, v175, v243\n\t" \
    "v_max_f32 v244, 0, v244\n\t" \
    "v_mfma_f32_32x32x16_bf16 v[192:207], v[212:215], v[72:75], v[192:207]\n\t" \
    "ds_read_b64_tr_b16 v[228:229], v247 offset:16896\n\t" \
    "ds_read_b64_tr_b16 v[230:231], v247 offset:17152\n\t" \
    "v_max_f32 v245, 0, v245\n\t" \
    "v_mul_f32 v244, v244, v244\n\t" \
    "v_mul_f32 v245, v245, v245\n\t" \
    "v_cvt_pk_bf16_f32 v239, v244, v245\n\t" \
    "buffer_store_dwordx4 v[236:239], v249, s[64:67], s68 offen offset:3072 sc1\n\t" \
    "s_waitcnt lgkmcnt(4)\n\t" \
    "v_mfma_f32_32x32x16_bf16 v[176:191], v[216:219], v[76:79], v[176:191]\n\t" \
    "ds_read_b64_tr_b16 v[208:209], v247 offset:18432\n\t" \
    "ds_read_b64_tr_b16 v[210:211], v247 offset:18688\n\t" \
    "v_mfma_f32_32x32x16_bf16 v[192:207], v[220:223], v[76:79], v[192:207]\n\t" \
    "ds_read_b64_tr_b16 v[212:213], v247 offset:18944\n\t" \
    "ds_read_b64_tr_b16 v[214:215], v247 offset:19200\n\t" \
    "s_waitcnt lgkmcnt(4)\n\t" \
    "v_mfma_f32_32x32x16_bf16 v[176:191], v[224:227], v[80:83], v[176:191]\n\t" \
    "ds_read_b64_tr_b16 v[216:217], v247 offset:20480\n\t" \
    "ds_read_b64_tr_b16 v[218:219], v247 offset:20736\n\t" \
    "v_mfma_f32_32x32x16_bf16 v[192:207], v[228:231], v[80:83], v[192:207]\n\t" \
    "ds_read_b64_tr_b16 v[220:221], v247 offset:20992\n\t" \
    "ds_read_b64_tr_b16 v[222:223], v247 offset:21248\n\t" \
    "s_waitcnt lgkmcnt(4)\n\t" \
    "v_mfma_f32_32x32x16_bf16 v[176:191], v[208:211], v[84:87], v[176:191]\n\t" \
    "ds_read_b64_tr_b16 v[224:225], v247 offset:22528\n\t" \
    "ds_read_b64_tr_b16 v[226:227], v247 offset:22784\n\t" \
    "v_mfma_f32_32x32x16_bf16 v[192:207], v[212:215], v[84:87], v[192:207]\n\t" \
    "ds_read_b64_tr_b16 v[228:229], v247 offset:23040\n\t" \
    "ds_read_b64_tr_b16 v[230:231], v247 offset:23296\n\t" \
    "s_waitcnt lgkmcnt(4)\n\t" \
    "v_mfma_f32_32x32x16_bf16 v[176:191], v[216:219], v[88:91], v[176:191]\n\t" \
    "ds_read_b64_tr_b16 v[208:209], v247 offset:24576\n\t" \
    "ds_read_b64_tr_b16 v[210:211], v247 offset:24832\n\t" \
    "v_mfma_f32_32x32x16_bf16 v[192:207], v[220:223], v[88:91], v[192:207]\n\t" \
    "ds_read_b64_tr_b16 v[212:213], v247 offset:25088\n\t" \
    "ds_read_b64_tr_b16 v[214:215], v247 offset:25344\n\t" \
    "s_waitcnt lgkmcnt(4)\n\t" \
    "v_mfma_f32_32x32x16_bf16 v[176:191], v[224:227], v[92:95], v[176:191]\n\t" \
    "ds_read_b64_tr_b16 v[216:217], v247 offset:26624\n\t" \
    "ds_read_b64_tr_b16 v[218:219], v247 offset:26880\n\t" \
    "v_mfma_f32_32x32x16_bf16 v[192:207], v[228:231], v[92:95], v[192:207]\n\t" \
    "ds_read_b64_tr_b16 v[220:221], v247 offset:27136\n\t" \
    "ds_read_b64_tr_b16 v[222:223], v247 offset:27392\n\t" \
    "s_waitcnt lgkmcnt(4)\n\t" \
    "v_mfma_f32_32x32x16_bf16 v[176:191], v[208:211], v[96:99], v[176:191]\n\t" \
    "ds_read_b64_tr_b16 v[224:225], v247 offset:28672\n\t" \
    "ds_read_b64_tr_b16 v[226:227], v247 offset:28928\n\t" \
    "v_mfma_f32_32x32x16_bf16 v[192:207], v[212:215], v[96:99], v[192:207]\n\t" \
    "ds_read_b64_tr_b16 v[228:229], v247 offset:29184\n\t" \
    "ds_read_b64_tr_b16 v[230:231], v247 offset:29440\n\t" \
    "s_waitcnt lgkmcnt(4)\n\t" \
    "v_mfma_f32_32x32x16_bf16 v[176:191], v[216:219], v[100:103], v[176:191]\n\t" \
    "ds_read_b64_tr_b16 v[208:209], v247 offset:30720\n\t" \
    "ds_read_b64_tr_b16 v[210:211], v247 offset:30976\n\t" \
    "v_mfma_f32_32x32x16_bf16 v[192:207], v[220:223], v[100:103], v[192:207]\n\t" \
    "ds_read_b64_tr_b16 v[212:213], v247 offset:31232\n\t" \
    "ds_read_b64_tr_b16 v[214:215], v247 offset:31488\n\t" \
    "s_waitcnt lgkmcnt(4)\n\t" \
    "v_mfma_f32_32x32x16_bf16 v[176:191], v[224:227], v[104:107], v[176:191]\n\t" \
    "ds_read_b64_tr_b16 v[216:217], v247 offset:32768\n\t" \
    "ds_read_b64_tr_b16 v[218:219], v247 offset:33024\n\t" \
    "v_mfma_f32_32x32x16_bf16 v[192:207], v[228:231], v[104:107], v[192:207]\n\t" \
    "ds_read_b64_tr_b16 v[220:221], v247 offset:33280\n\t" \
    "ds_read_b64_tr_b16 v[222:223], v247 offset:33536\n\t" \
    "s_waitcnt lgkmcnt(4)\n\t" \
    "v_mfma_f32_32x32x16_bf16 v[176:191], v[208:211], v[108:111], v[176:191]\n\t" \
    "ds_read_b64_tr_b16 v[224:225], v247 offset:34816\n\t" \
    "ds_read_b64_tr_b16 v[226:227], v247 offset:35072\n\t" \
    "v_mfma_f32_32x32x16_bf16 v[192:207], v[212:215], v[108:111], v[192:207]\n\t" \
    "ds_read_b64_tr_b16 v[228:229], v247 offset:35328\n\t" \
    "ds_read_b64_tr_b16 v[230:231], v247 offset:35584\n\t" \
    "s_waitcnt lgkmcnt(4)\n\t" \
    "v_mfma_f32_32x32x16_bf16 v[176:191], v[216:219], v[112:115], v[176:191]\n\t" \
    "ds_read_b64_tr_b16 v[208:209], v247 offset:36864\n\t" \
    "ds_read_b64_tr_b16 v[210:211], v247 offset:37120\n\t" \
    "v_mfma_f32_32x32x16_bf16 v[192:207], v[220:223], v[112:115], v[192:207]\n\t" \
    "ds_read_b64_tr_b16 v[212:213], v247 offset:37376\n\t" \
    "ds_read_b64_tr_b16 v[214:215], v247 offset:37632\n\t" \
    "s_waitcnt lgkmcnt(4)\n\t" \
    "v_mfma_f32_32x32x16_bf16 v[176:191], v[224:227], v[116:119], v[176:191]\n\t" \
    "ds_read_b64_tr_b16 v[216:217], v247 offset:38912\n\t" \
    "ds_read_b64_tr_b16 v[218:219], v247 offset:39168\n\t" \
    "v_mfma_f32_32x32x16_bf16 v[192:207], v[228:231], v[116:119], v[192:207]\n\t" \
    "ds_read_b64_tr_b16 v[220:221], v247 offset:39424\n\t" \
    "ds_read_b64_tr_b16 v[222:223], v247 offset:39680\n\t" \
    "s_waitcnt lgkmcnt(4)\n\t" \
    "v_mfma_f32_32x32x16_bf16 v[176:191], v[208:211], v[120:123], v[176:191]\n\t" \
    "s_waitcnt vmcnt(0)\n\t" \
    "s_barrier\n\t" \
    "ds_read_b64_tr_b16 v[224:225], v247 offset:40960\n\t" \
    "ds_read_b64_tr_b16 v[226:227], v247 offset:41216\n\t" \
    "v_mfma_f32_32x32x16_bf16 v[192:207], v[212:215], v[120:123], v[192:207]\n\t" \
    "ds_read_b64_tr_b16 v[228:229], v247 offset:41472\n\t" \
    "ds_read_b64_tr_b16 v[230:231], v247 offset:41728\n\t" \
    "s_waitcnt lgkmcnt(4)\n\t" \
    "v_mfma_f32_32x32x16_bf16 v[176:191], v[216:219], v[124:127], v[176:191]\n\t" \
    "ds_read_b64_tr_b16 v[208:209], v247 offset:43008\n\t" \
    "ds_read_b64_tr_b16 v[210:211], v247 offset:43264\n\t" \
    "v_mfma_f32_32x32x16_bf16 v[192:207], v[220:223], v[124:127], v[192:207]\n\t" \
    "ds_read_b64_tr_b16 v[212:213], v247 offset:43520\n\t" \
    "ds_read_b64_tr_b16 v[214:215], v247 offset:43776\n\t" \
    "s_waitcnt lgkmcnt(4)\n\t" \
    "v_mfma_f32_32x32x16_bf16 v[176:191], v[224:227], v[128:131], v[176:191]\n\t" \
    "ds_read_b64_tr_b16 v[216:217], v247 offset:45056\n\t" \
    "ds_read_b64_tr_b16 v[218:219], v247 offset:45312\n\t" \
    "v_mfma_f32_32x32x16_bf16 v[192:207], v[228:231], v[128:131], v[192:207]\n\t" \
    "ds_read_b64_tr_b16 v[220:221], v247 offset:45568\n\t" \
    "ds_read_b64_tr_b16 v[222:223], v247 offset:45824\n\t" \
    "s_waitcnt lgkmcnt(4)\n\t" \
    "v_mfma_f32_32x32x16_bf16 v[176:191], v[208:211], v[132:135], v[176:191]\n\t" \
    "ds_read_b64_tr_b16 v[224:225], v247 offset:47104\n\t" \
    "ds_read_b64_tr_b16 v[226:227], v247 offset:47360\n\t" \
    "v_mfma_f32_32x32x16_bf16 v[192:207], v[212:215], v[132:135], v[192:207]\n\t" \
    "ds_read_b64_tr_b16 v[228:229], v247 offset:47616\n\t" \
    "ds_read_b64_tr_b16 v[230:231], v247 offset:47872\n\t" \
    "s_waitcnt lgkmcnt(4)\n\t" \
    "v_mfma_f32_32x32x16_bf16 v[176:191], v[216:219], v[136:139], v[176:191]\n\t" \
    "v_add_u32 v247, 0xfffe8000, v247\n\t" \
    "ds_read_b64_tr_b16 v[208:209], v247 offset:0\n\t" \
    "ds_read_b64_tr_b16 v[210:211], v247 offset:256\n\t" \
    "v_mfma_f32_32x32x16_bf16 v[192:207], v[220:223], v[136:139], v[192:207]\n\t" \
    "ds_read_b64_tr_b16 v[212:213], v247 offset:512\n\t" \
    "ds_read_b64_tr_b16 v[214:215], v247 offset:768\n\t" \
    "s_waitcnt lgkmcnt(4)\n\t" \
    "v_mfma_f32_32x32x16_bf16 v[176:191], v[224:227], v[140:143], v[176:191]\n\t" \
    "ds_read_b64_tr_b16 v[216:217], v247 offset:2048\n\t" \
    "ds_read_b64_tr_b16 v[218:219], v247 offset:2304\n\t" \
    "v_mfma_f32_32x32x16_bf16 v[192:207], v[228:231], v[140:143], v[192:207]\n\t" \
    "ds_read_b64_tr_b16 v[220:221], v247 offset:2560\n\t" \
    "ds_read_b64_tr_b16 v[222:223], v247 offset:2816\n\t" \
    "s_mov_b32 s68, s71\n\t" \
    "s_add_u32 s71, s71, 4096\n\t" \
    "s_mov_b32 s66, s69\n\t" \
    "v_add_u32 v248, 0x100, v248\n\t" \
    "s_sub_u32 s70, s70, 1\n\t" \
    "s_cmp_eq_u32 s70, 0\n\t" \
    "s_cbranch_scc1 .Lp1end_%=\n\t" \
    "s_waitcnt lgkmcnt(4)\n\t" \
    "v_mfma_f32_32x32x16_bf16 v[144:159], v[208:211], v[0:3], 0\n\t" \
    "ds_read_b64_tr_b16 v[224:225], v247 offset:4096\n\t" \
    "ds_read_b64_tr_b16 v[226:227], v247 offset:4352\n\t" \
    "ds_read_b128 v[232:235], v248 offset:0\n\t" \
    "v_mfma_f32_32x32x16_bf16 v[160:175], v[212:215], v[0:3], 0\n\t" \
    "ds_read_b64_tr_b16 v[228:229], v247 offset:4608\n\t" \
    "ds_read_b64_tr_b16 v[230:231], v247 offset:4864\n\t" \
    "s_waitcnt lgkmcnt(5)\n\t" \
    "v_mfma_f32_32x32x16_bf16 v[144:159], v[216:219], v[4:7], v[144:159]\n\t" \
    "ds_read_b64_tr_b16 v[208:209], v247 offset:6144\n\t" \
    "ds_read_b64_tr_b16 v[210:211], v247 offset:6400\n\t" \
    "ds_read_b128 v[240:243], v248 offset:32\n\t" \
    "v_mfma_f32_32x32x16_bf16 v[160:175], v[220:223], v[4:7], v[160:175]\n\t" \
    "ds_read_b64_tr_b16 v[212:213], v247 offset:6656\n\t" \
    "ds_read_b64_tr_b16 v[214:215], v247 offset:6912\n\t" \
    "s_waitcnt lgkmcnt(5)\n\t" \
    "v_mfma_f32_32x32x16_bf16 v[144:159], v[224:227], v[8:11], v[144:159]\n\t" \
    "ds_read_b64_tr_b16 v[216:217], v247 offset:8192\n\t" \
    "ds_read_b64_tr_b16 v[218:219], v247 offset:8448\n\t" \
    "v_mfma_f32_32x32x16_bf16 v[160:175], v[228:231], v[8:11], v[160:175]\n\t" \
    "ds_read_b64_tr_b16 v[220:221], v247 offset:8704\n\t" \
    "ds_read_b64_tr_b16 v[222:223], v247 offset:8960\n\t" \
    "s_waitcnt lgkmcnt(4)\n\t" \
    "v_mfma_f32_32x32x16_bf16 v[144:159], v[208:211], v[12:15], v[144:159]\n\t" \
    "ds_read_b64_tr_b16 v[224:225], v247 offset:10240\n\t" \
    "ds_read_b64_tr_b16 v[226:227], v247 offset:10496\n\t" \
    "v_add_f32 v244, v176, v232\n\t" \
    "v_add_f32 v245, v177, v233\n\t" \
    "v_max_f32 v244, 0, v244\n\t" \
    "v_mfma_f32_32x32x16_bf16 v[160:175], v[212:215], v[12:15], v[160:175]\n\t" \
    "ds_read_b64_tr_b16 v[228:229], v247 offset:10752\n\t" \
    "ds_read_b64_tr_b16 v[230:231], v247 offset:11008\n\t" \
    "v_max_f32 v245, 0, v245\n\t" \
    "v_mul_f32 v244, v244, v244\n\t" \
    "v_mul_f32 v245, v245, v245\n\t" \
    "v_cvt_pk_bf16_f32 v236, v244, v245\n\t" \
    "s_waitcnt lgkmcnt(4)\n\t" \
    "v_mfma_f32_32x32x16_bf16 v[144:159], v[216:219], v[16:19], v[144:159]\n\t" \
    "ds_read_b64_tr_b16 v[208:209], v247 offset:12288\n\t" \
    "ds_read_b64_tr_b16 v[210:211], v247 offset:12544\n\t" \
    "v_add_f32 v244, v178, v234\n\t" \
    "v_add_f32 v245, v179, v235\n\t" \
    "ds_read_b128 v[232:235], v248 offset:64\n\t" \
    "v_max_f32 v244, 0, v244\n\t" \
    "v_mfma_f32_32x32x16_bf16 v[160:175], v[220:223], v[16:19], v[160:175]\n\t" \
    "ds_read_b64_tr_b16 v[212:213], v247 offset:12800\n\t" \
    "ds_read_b64_tr_b16 v[214:215], v247 offset:13056\n\t" \
    "v_max_f32 v245, 0, v245\n\t" \
    "v_mul_f32 v244, v244, v244\n\t" \
    "v_mul_f32 v245, v245, v245\n\t" \
    "v_cvt_pk_bf16_f32 v237, v244, v245\n\t" \
    "s_waitcnt lgkmcnt(5)\n\t" \
    "v_mfma_f32_32x32x16_bf16 v[144:159], v[224:227], v[20:23], v[144:159]\n\t" \
    "ds_read_b64_tr_b16 v[216:217], v247 offset:14336\n\t" \
    "ds_read_b64_tr_b16 v[218:219], v247 offset:14592\n\t" \
    "v_add_f32 v244, v180, v240\n\t" \
    "v_add_f32 v245, v181, v241\n\t" \
    "v_max_f32 v244, 0, v244\n\t" \
    "v_mfma_f32_32x32x16_bf16 v[160:175], v[228:231], v[20:23], v[160:175]\n\t" \
    "ds_read_b64_tr_b16 v[220:221], v247 offset:14848\n\t" \
    "ds_read_b64_tr_b16 v[222:223], v247 offset:15104\n\t" \
    "v_max_f32 v245, 0, v245\n\t" \
    "v_mul_f32 v244, v244, v244\n\t" \
    "v_mul_f32 v245, v245, v245\n\t" \
    "v_cvt_pk_bf16_f32 v238, v244, v245\n\t" \
    "s_waitcnt lgkmcnt(4)\n\t" \
    "v_mfma_f32_32x32x16_bf16 v[144:159], v[208:211], v[24:27], v[144:159]\n\t" \
    "s_barrier\n\t" \
    "ds_read_b64_tr_b16 v[224:225], v247 offset:16384\n\t" \
    "ds_read_b64_tr_b16 v[226:227], v247 offset:16640\n\t" \
    "v_add_f32 v244, v182, v242\n\t" \
    "v_add_f32 v245, v183, v243\n\t" \
    "ds_read_b128 v[240:243], v248 offset:96\n\t" \
    "v_max_f32 v244, 0, v244\n\t" \
    "v_mfma_f32_32x32x16_bf16 v[160:175], v[212:215], v[24:27], v[160:175]\n\t" \
    "ds_read_b64_tr_b16 v[228:229], v247 offset:16896\n\t" \
    "ds_read_b64_tr_b16 v[230:231], v247 offset:17152\n\t" \
    "v_max_f32 v245, 0, v245\n\t" \
    "v_mul_f32 v244, v244, v244\n\t" \
    "v_mul_f32 v245, v245, v245\n\t" \
    "v_cvt_pk_bf16_f32 v239, v244, v245\n\t" \
    "buffer_store_dwordx4 v[236:239], v249, s[64:67], s68 offen offset:0 sc1\n\t" \
    "s_waitcnt lgkmcnt(5)\n\t" \
    "v_mfma_f32_32x32x16_bf16 v[144:159], v[216:219], v[28:31], v[144:159]\n\t" \
    "ds_read_b64_tr_b16 v[208:209], v247 offset:18432\n\t" \
    "ds_read_b64_tr_b16 v[210:211], v247 offset:18688\n\t" \
    "v_add_f32 v244, v184, v232\n\t" \
    "v_add_f32 v245, v185, v233\n\t" \
    "v_max_f32 v244, 0, v244\n\t" \
    "v_mfma_f32_32x32x16_bf16 v[160:175], v[220:223], v[28:31], v[160:175]\n\t" \
    "ds_read_b64_tr_b16 v[212:213], v247 offset:18944\n\t" \
    "ds_read_b64_tr_b16 v[214:215], v247 offset:19200\n\t" \
    "v_max_f32 v245, 0, v245\n\t" \
    "v_mul_f32 v244, v244, v244\n\t" \
    "v_mul_f32 v245, v245, v245\n\t" \
    "v_cvt_pk_bf16_f32 v236, v244, v245\n\t" \
    "s_waitcnt lgkmcnt(4)\n\t" \
    "v_mfma_f32_32x32x16_bf16 v[144:159], v[224:227], v[32:35], v[144:159]\n\t" \
    "ds_read_b64_tr_b16 v[216:217], v247 offset:20480\n\t" \
    "ds_read_b64_tr_b16 v[218:219], v247 offset:20736\n\t" \
    "v_add_f32 v244, v186, v234\n\t" \
    "v_add_f32 v245, v187, v235\n\t" \
    "ds_read_b128 v[232:235], v248 offset:128\n\t" \
    "v_max_f32 v244, 0, v244\n\t" \
    "v_mfma_f32_32x32x16_bf16 v[160:175], v[228:231], v[32:35], v[160:175]\n\t" \
    "ds_read_b64_tr_b16 v[220:221], v247 offset:20992\n\t" \
    "ds_read_b64_tr_b16 v[222:223], v247 offset:21248\n\t" \
    "v_max_f32 v245, 0, v245\n\t" \
    "v_mul_f32 v244, v244, v244\n\t" \
    "v_mul_f32 v245, v245, v245\n\t" \
    "v_cvt_pk_bf16_f32 v237, v244, v245\n\t" \
    "s_waitcnt lgkmcnt(5)\n\t" \
    "v_mfma_f32_32x32x16_bf16 v[144:159], v[208:211], v[36:39], v[144:159]\n\t" \
    "ds_read_b64_tr_b16 v[224:225], v247 offset:22528\n\t" \
    "ds_read_b64_tr_b16 v[226:227], v247 offset:22784\n\t" \
    "v_add_f32 v244, v188, v240\n\t" \
    "v_add_f32 v245, v189, v241\n\t" \
    "v_max_f32 v244, 0, v244\n\t" \
    "v_mfma_f32_32x32x16_bf16 v[160:175], v[212:215], v[36:39], v[160:175]\n\t" \
    "ds_read_b64_tr_b16 v[228:229], v247 offset:23040\n\t" \
    "ds_read_b64_tr_b16 v[230:231], v247 offset:23296\n\t" \
    "v_max_f32 v245, 0, v245\n\t" \
    "v_mul_f32 v244, v244, v244\n\t" \
    "v_mul_f32 v245, v245, v245\n\t" \
    "v_cvt_pk_bf16_f32 v238, v244, v245\n\t" \
    "s_waitcnt lgkmcnt(4)\n\t" \
    "v_mfma_f32_32x32x16_bf16 v[144:159], v[216:219], v[40:43], v[144:159]\n\t" \
    "ds_read_b64_tr_b16 v[208:209], v247 offset:24576\n\t" \
    "ds_read_b64_tr_b16 v[210:211], v247 offset:24832\n\t" \
    "v_add_f32 v244, v190, v242\n\t" \
    "v_add_f32 v245, v191, v243\n\t" \
    "ds_read_b128 v[240:243], v248 offset:160\n\t" \
    "v_max_f32 v244, 0, v244\n\t" \
    "v_mfma_f32_32x32x16_bf16 v[160:175], v[220:223], v[40:43], v[160:175]\n\t" \
    "ds_read_b64_tr_b16 v[212:213], v247 offset:25088\n\t" \
    "ds_read_b64_tr_b16 v[214:215], v247 offset:25344\n\t" \
    "v_max_f32 v245, 0, v245\n\t" \
    "v_mul_f32 v244, v244, v244\n\t" \
    "v_mul_f32 v245, v245, v245\n\t" \
    "v_cvt_pk_bf16_f32 v239, v244, v245\n\t" \
    "buffer_store_dwordx4 v[236:239], v249, s[64:67], s68 offen offset:1024 sc1\n\t" \
    "s_waitcnt lgkmcnt(5)\n\t" \
    "v_mfma_f32_32x32x16_bf16 v[144:159], v[224:227], v[44:47], v[144:159]\n\t" \
    "ds_read_b64_tr_b16 v[216:217], v247 offset:26624\n\t" \
    "ds_read_b64_tr_b16 v[218:219], v247 offset:26880\n\t" \
    "v_add_f32 v244, v192, v232\n\t" \
    "v_add_f32 v245, v193, v233\n\t" \
    "v_max_f32 v244, 0, v244\n\t" \
    "v_mfma_f32_32x32x16_bf16 v[160:175], v[228:231], v[44:47], v[160:175]\n\t" \
    "ds_read_b64_tr_b16 v[220:221], v247 offset:27136\n\t" \
    "ds_read_b64_tr_b16 v[222:223], v247 offset:27392\n\t" \
    "v_max_f32 v245, 0, v245\n\t" \
    "v_mul_f32 v244, v244, v244\n\t" \
    "v_mul_f32 v245, v245, v245\n\t" \
    "v_cvt_pk_bf16_f32 v236, v244, v245\n\t" \
    "s_waitcnt lgkmcnt(4)\n\t" \
    "v_mfma_f32_32x32x16_bf16 v[144:159], v[208:211], v[48:51], v[144:159]\n\t" \
    "ds_read_b64_tr_b16 v[224:225], v247 offset:28672\n\t" \
    "ds_read_b64_tr_b16 v[226:227], v247 offset:28928\n\t" \
    "v_add_f32 v244, v194, v234\n\t" \
    "v_add_f32 v245, v195, v235\n\t" \
    "ds_read_b128 v[232:235], v248 offset:192\n\t" \
    "v_max_f32 v244, 0, v244\n\t" \
    "v_mfma_f32_32x32x16_bf16 v[160:175], v[212:215], v[48:51], v[160:175]\n\t" \
    "ds_read_b64_tr_b16 v[228:229], v247 offset:29184\n\t" \
    "ds_read_b64_tr_b16 v[230:231], v247 offset:29440\n\t" \
    "v_max_f32 v245, 0, v245\n\t" \
    "v_mul_f32 v244, v244, v244\n\t" \
    "v_mul_f32 v245, v245, v245\n\t" \
    "v_cvt_pk_bf16_f32 v237, v244, v245\n\t" \
    "s_waitcnt lgkmcnt(5)\n\t" \
    "v_mfma_f32_32x32x16_bf16 v[144:159], v[216:219], v[52:55], v[144:159]\n\t" \
    "ds_read_b64_tr_b16 v[208:209], v247 offset:30720\n\t" \
    "ds_read_b64_tr_b16 v[210:211], v247 offset:30976\n\t" \
    "v_add_f32 v244, v196, v240\n\t" \
    "v_add_f32 v245, v197, v241\n\t" \
    "v_max_f32 v244, 0, v244\n\t" \
    "v_mfma_f32_32x32x16_bf16 v[160:175], v[220:223], v[52:55], v[160:175]\n\t" \
    "ds_read_b64_tr_b16 v[212:213], v247 offset:31232\n\t" \
    "ds_read_b64_tr_b16 v[214:215], v247 offset:31488\n\t" \
    "v_max_f32 v245, 0, v245\n\t" \
    "v_mul_f32 v244, v244, v244\n\t" \
    "v_mul_f32 v245, v245, v245\n\t" \
    "v_cvt_pk_bf16_f32 v238, v244, v245\n\t" \
    "s_waitcnt lgkmcnt(4)\n\t" \
    "v_mfma_f32_32x32x16_bf16 v[144:159], v[224:227], v[56:59], v[144:159]\n\t" \
    "ds_read_b64_tr_b16 v[216:217], v247 offset:32768\n\t" \
    "ds_read_b64_tr_b16 v[218:219], v247 offset:33024\n\t" \
    "v_add_f32 v244, v198, v242\n\t" \
    "v_add_f32 v245, v199, v243\n\t" \
    "ds_read_b128 v[240:243], v248 offset:224\n\t" \
    "v_max_f32 v244, 0, v244\n\t" \
    "v_mfma_f32_32x32x16_bf16 v[160:175], v[228:231], v[56:59], v[160:175]\n\t" \
    "ds_read_b64_tr_b16 v[220:221], v247 offset:33280\n\t" \
    "ds_read_b64_tr_b16 v[222:223], v247 offset:33536\n\t" \
    "v_max_f32 v245, 0, v245\n\t" \
    "v_mul_f32 v244, v244, v244\n\t" \
    "v_mul_f32 v245, v245, v245\n\t" \
    "v_cvt_pk_bf16_f32 v239, v244, v245\n\t" \
    "buffer_store_dwordx4 v[236:239], v249, s[64:67], s68 offen offset:2048 sc1\n\t" \
    "s_waitcnt lgkmcnt(5)\n\t" \
    "v_mfma_f32_32x32x16_bf16 v[144:159], v[208:211], v[60:63], v[144:159]\n\t" \
    "ds_read_b64_tr_b16 v[224:225], v247 offset:34816\n\t" \
    "ds_read_b64_tr_b16 v[226:227], v247 offset:35072\n\t" \
    "v_add_f32 v244, v200, v232\n\t" \
    "v_add_f32 v245, v201, v233\n\t" \
    "v_max_f32 v244, 0, v244\n\t" \
    "v_mfma_f32_32x32x16_bf16 v[160:175], v[212:215], v[60:63], v[160:175]\n\t" \
    "ds_read_b64_tr_b16 v[228:229], v247 offset:35328\n\t" \
    "ds_read_b64_tr_b16 v[230:231], v247 offset:35584\n\t" \
    "v_max_f32 v245, 0, v245\n\t" \
    "v_mul_f32 v244, v244, v244\n\t" \
    "v_mul_f32 v245, v245, v245\n\t" \
    "v_cvt_pk_bf16_f32 v236, v244, v245\n\t" \
    "s_waitcnt lgkmcnt(4)\n\t" \
    "v_mfma_f32_32x32x16_bf16 v[144:159], v[216:219], v[64:67], v[144:159]\n\t" \
    "ds_read_b64_tr_b16 v[208:209], v247 offset:36864\n\t" \
    "ds_read_b64_tr_b16 v[210:211], v247 offset:37120\n\t" \
    "v_add_f32 v244, v202, v234\n\t" \
    "v_add_f32 v245, v203, v235\n\t" \
    "v_max_f32 v244, 0, v244\n\t" \
    "v_mfma_f32_32x32x16_bf16 v[160:175], v[220:223], v[64:67], v[160:175]\n\t" \
    "ds_read_b64_tr_b16 v[212:213], v247 offset:37376\n\t" \
    "ds_read_b64_tr_b16 v[214:215], v247 offset:37632\n\t" \
    "v_max_f32 v245, 0, v245\n\t" \
    "v_mul_f32 v244, v244, v244\n\t" \
    "v_mul_f32 v245, v245, v245\n\t" \
    "v_cvt_pk_bf16_f32 v237, v244, v245\n\t" \
    "s_waitcnt lgkmcnt(4)\n\t" \
    "v_mfma_f32_32x32x16_bf16 v[144:159], v[224:227], v[68:71], v[144:159]\n\t" \
    "ds_read_b64_tr_b16 v[216:217], v247 offset:38912\n\t" \
    "ds_read_b64_tr_b16 v[218:219], v247 offset:39168\n\t" \
    "v_add_f32 v244, v204, v240\n\t" \
    "v_add_f32 v245, v205, v241\n\t" \
    "v_max_f32 v244, 0, v244\n\t" \
    "v_mfma_f32_32x32x16_bf16 v[160:175], v[228:231], v[68:71], v[160:175]\n\t" \
    "ds_read_b64_tr_b16 v[220:221], v247 offset:39424\n\t" \
    "ds_read_b64_tr_b16 v[222:223], v247 offset:39680\n\t" \
    "v_max_f32 v245, 0, v245\n\t" \
    "v_mul_f32 v244, v244, v244\n\t" \
    "v_mul_f32 v245, v245, v245\n\t" \
    "v_cvt_pk_bf16_f32 v238, v244, v245\n\t" \
    "s_waitcnt lgkmcnt(4)\n\t" \
    "v_mfma_f32_32x32x16_bf16 v[144:159], v[208:211], v[72:75], v[144:159]\n\t" \
    "s_barrier\n\t" \
    "ds_read_b64_tr_b16 v[224:225], v247 offset:40960\n\t" \
    "ds_read_b64_tr_b16 v[226:227], v247 offset:41216\n\t" \
    "v_add_f32 v244, v206, v242\n\t" \
    "v_add_f32 v245, v207, v243\n\t" \
    "v_max_f32 v244, 0, v244\n\t" \
    "v_mfma_f32_32x32x16_bf16 v[160:175], v[212:215], v[72:75], v[160:175]\n\t" \
    "ds_read_b64_tr_b16 v[228:229], v247 offset:41472\n\t" \
    "ds_read_b64_tr_b16 v[230:231], v247 offset:41728\n\t" \
    "v_max_f32 v245, 0, v245\n\t" \
    "v_mul_f32 v244, v244, v244\n\t" \
    "v_mul_f32 v245, v245, v245\n\t" \
    "v_cvt_pk_bf16_f32 v239, v244, v245\n\t" \
    "buffer_store_dwordx4 v[236:239], v249, s[64:67], s68 offen offset:3072 sc1\n\t" \
    "s_waitcnt lgkmcnt(4)\n\t" \
    "v_mfma_f32_32x32x16_bf16 v[144:159], v[216:219], v[76:79], v[144:159]\n\t" \
    "ds_read_b64_tr_b16 v[208:209], v247 offset:43008\n\t" \
    "ds_read_b64_tr_b16 v[210:211], v247 offset:43264\n\t" \
    "v_mfma_f32_32x32x16_bf16 v[160:175], v[220:223], v[76:79], v[160:175]\n\t" \
    "ds_read_b64_tr_b16 v[212:213], v247 offset:43520\n\t" \
    "ds_read_b64_tr_b16 v[214:215], v247 offset:43776\n\t" \
    "s_waitcnt lgkmcnt(4)\n\t" \
    "v_mfma_f32_32x32x16_bf16 v[144:159], v[224:227], v[80:83], v[144:159]\n\t" \
    "ds_read_b64_tr_b16 v[216:217], v247 offset:45056\n\t" \
    "ds_read_b64_tr_b16 v[218:219], v247 offset:45312\n\t" \
    "v_mfma_f32_32x32x16_bf16 v[160:175], v[228:231], v[80:83], v[160:175]\n\t" \
    "ds_read_b64_tr_b16 v[220:221], v247 offset:45568\n\t" \
    "ds_read_b64_tr_b16 v[222:223], v247 offset:45824\n\t" \
    "s_waitcnt lgkmcnt(4)\n\t" \
    "v_mfma_f32_32x32x16_bf16 v[144:159], v[208:211], v[84:87], v[144:159]\n\t" \
    "ds_read_b64_tr_b16 v[224:225], v247 offset:47104\n\t" \
    "ds_read_b64_tr_b16 v[226:227], v247 offset:47360\n\t" \
    "v_mfma_f32_32x32x16_bf16 v[160:175], v[212:215], v[84:87], v[160:175]\n\t" \
    "ds_read_b64_tr_b16 v[228:229], v247 offset:47616\n\t" \
    "ds_read_b64_tr_b16 v[230:231], v247 offset:47872\n\t" \
    "s_waitcnt lgkmcnt(4)\n\t" \
    "v_mfma_f32_32x32x16_bf16 v[144:159], v[216:219], v[88:91], v[144:159]\n\t" \
    "v_add_u32 v247, 0xc000, v247\n\t" \
    "ds_read_b64_tr_b16 v[208:209], v247 offset:0\n\t" \
    "ds_read_b64_tr_b16 v[210:211], v247 offset:256\n\t" \
    "v_mfma_f32_32x32x16_bf16 v[160:175], v[220:223], v[88:91], v[160:175]\n\t" \
    "ds_read_b64_tr_b16 v[212:213], v247 offset:512\n\t" \
    "ds_read_b64_tr_b16 v[214:215], v247 offset:768\n\t" \
    "s_waitcnt lgkmcnt(4)\n\t" \
    "v_mfma_f32_32x32x16_bf16 v[144:159], v[224:227], v[92:95], v[144:159]\n\t" \
    "ds_read_b64_tr_b16 v[216:217], v247 offset:2048\n\t" \
    "ds_read_b64_tr_b16 v[218:219], v247 offset:2304\n\t" \
    "v_mfma_f32_32x32x16_bf16 v[160:175], v[228:231], v[92:95], v[160:175]\n\t" \
    "ds_read_b64_tr_b16 v[220:221], v247 offset:2560\n\t" \
    "ds_read_b64_tr_b16 v[222:223], v247 offset:2816\n\t" \
    "s_waitcnt lgkmcnt(4)\n\t" \
    "v_mfma_f32_32x32x16_bf16 v[144:159], v[208:211], v[96:99], v[144:159]\n\t" \
    "ds_read_b64_tr_b16 v[224:225], v247 offset:4096\n\t" \
    "ds_read_b64_tr_b16 v[226:227], v247 offset:4352\n\t" \
    "v_mfma_f32_32x32x16_bf16 v[160:175], v[212:215], v[96:99], v[160:175]\n\t" \
    "ds_read_b64_tr_b16 v[228:229], v247 offset:4608\n\t" \
    "ds_read_b64_tr_b16 v[230:231], v247 offset:4864\n\t" \
    "s_waitcnt lgkmcnt(4)\n\t" \
    "v_mfma_f32_32x32x16_bf16 v[144:159], v[216:219], v[100:103], v[144:159]\n\t" \
    "ds_read_b64_tr_b16 v[208:209], v247 offset:6144\n\t" \
    "ds_read_b64_tr_b16 v[210:211], v247 offset:6400\n\t" \
    "v_mfma_f32_32x32x16_bf16 v[160:175], v[220:223], v[100:103], v[160:175]\n\t" \
    "ds_read_b64_tr_b16 v[212:213], v247 offset:6656\n\t" \
    "ds_read_b64_tr_b16 v[214:215], v247 offset:6912\n\t" \
    "s_waitcnt lgkmcnt(4)\n\t" \
    "v_mfma_f32_32x32x16_bf16 v[144:159], v[224:227], v[104:107], v[144:159]\n\t" \
    "ds_read_b64_tr_b16 v[216:217], v247 offset:8192\n\t" \
    "ds_read_b64_tr_b16 v[218:219], v247 offset:8448\n\t" \
    "v_mfma_f32_32x32x16_bf16 v[160:175], v[228:231], v[104:107], v[160:175]\n\t" \
    "ds_read_b64_tr_b16 v[220:221], v247 offset:8704\n\t" \
    "ds_read_b64_tr_b16 v[222:223], v247 offset:8960\n\t" \
    "s_waitcnt lgkmcnt(4)\n\t" \
    "v_mfma_f32_32x32x16_bf16 v[144:159], v[208:211], v[108:111], v[144:159]\n\t" \
    "ds_read_b64_tr_b16 v[224:225], v247 offset:10240\n\t" \
    "ds_read_b64_tr_b16 v[226:227], v247 offset:10496\n\t" \
    "v_mfma_f32_32x32x16_bf16 v[160:175], v[212:215], v[108:111], v[160:175]\n\t" \
    "ds_read_b64_tr_b16 v[228:229], v247 offset:10752\n\t" \
    "ds_read_b64_tr_b16 v[230:231], v247 offset:11008\n\t" \
    "s_waitcnt lgkmcnt(4)\n\t" \
    "v_mfma_f32_32x32x16_bf16 v[144:159], v[216:219], v[112:115], v[144:159]\n\t" \
    "ds_read_b64_tr_b16 v[208:209], v247 offset:12288\n\t" \
    "ds_read_b64_tr_b16 v[210:211], v247 offset:12544\n\t" \
    "v_mfma_f32_32x32x16_bf16 v[160:175], v[220:223], v[112:115], v[160:175]\n\t" \
    "ds_read_b64_tr_b16 v[212:213], v247 offset:12800\n\t" \
    "ds_read_b64_tr_b16 v[214:215], v247 offset:13056\n\t" \
    "s_waitcnt lgkmcnt(4)\n\t" \
    "v_mfma_f32_32x32x16_bf16 v[144:159], v[224:227], v[116:119], v[144:159]\n\t" \
    "ds_read_b64_tr_b16 v[216:217], v247 offset:14336\n\t" \
    "ds_read_b64_tr_b16 v[218:219], v247 offset:14592\n\t" \
    "v_mfma_f32_32x32x16_bf16 v[160:175], v[228:231], v[116:119], v[160:175]\n\t" \
    "ds_read_b64_tr_b16 v[220:221], v247 offset:14848\n\t" \
    "ds_read_b64_tr_b16 v[222:223], v247 offset:15104\n\t" \
    "s_waitcnt lgkmcnt(4)\n\t" \
    "v_mfma_f32_32x32x16_bf16 v[144:159], v[208:211], v[120:123], v[144:159]\n\t" \
    "s_barrier\n\t" \
    "ds_read_b64_tr_b16 v[224:225], v247 offset:16384\n\t" \
    "ds_read_b64_tr_b16 v[226:227], v247 offset:16640\n\t" \
    "v_mfma_f32_32x32x16_bf16 v[160:175], v[212:215], v[120:123], v[160:175]\n\t" \
    "ds_read_b64_tr_b16 v[228:229], v247 offset:16896\n\t" \
    "ds_read_b64_tr_b16 v[230:231], v247 offset:17152\n\t" \
    "s_waitcnt lgkmcnt(4)\n\t" \
    "v_mfma_f32_32x32x16_bf16 v[144:159], v[216:219], v[124:127], v[144:159]\n\t" \
    "ds_read_b64_tr_b16 v[208:209], v247 offset:18432\n\t" \
    "ds_read_b64_tr_b16 v[210:211], v247 offset:18688\n\t" \
    "v_mfma_f32_32x32x16_bf16 v[160:175], v[220:223], v[124:127], v[160:175]\n\t" \
    "ds_read_b64_tr_b16 v[212:213], v247 offset:18944\n\t" \
    "ds_read_b64_tr_b16 v[214:215], v247 offset:19200\n\t" \
    "s_waitcnt lgkmcnt(4)\n\t" \
    "v_mfma_f32_32x32x16_bf16 v[144:159], v[224:227], v[128:131], v[144:159]\n\t" \
    "ds_read_b64_tr_b16 v[216:217], v247 offset:20480\n\t" \
    "ds_read_b64_tr_b16 v[218:219], v247 offset:20736\n\t" \
    "v_mfma_f32_32x32x16_bf16 v[160:175], v[228:231], v[128:131], v[160:175]\n\t" \
    "ds_read_b64_tr_b16 v[220:221], v247 offset:20992\n\t" \
    "ds_read_b64_tr_b16 v[222:223], v247 offset:21248\n\t" \
    "s_waitcnt lgkmcnt(4)\n\t" \
    "v_mfma_f32_32x32x16_bf16 v[144:159], v[208:211], v[132:135], v[144:159]\n\t" \
    "ds_read_b64_tr_b16 v[224:225], v247 offset:22528\n\t" \
    "ds_read_b64_tr_b16 v[226:227], v247 offset:22784\n\t" \
    "v_mfma_f32_32x32x16_bf16 v[160:175], v[212:215], v[132:135], v[160:175]\n\t" \
    "ds_read_b64_tr_b16 v[228:229], v247 offset:23040\n\t" \
    "ds_read_b64_tr_b16 v[230:231], v247 offset:23296\n\t" \
    "s_waitcnt lgkmcnt(4)\n\t" \
    "v_mfma_f32_32x32x16_bf16 v[144:159], v[216:219], v[136:139], v[144:159]\n\t" \
    "ds_read_b64_tr_b16 v[208:209], v247 offset:24576\n\t" \
    "ds_read_b64_tr_b16 v[210:211], v247 offset:24832\n\t" \
    "v_mfma_f32_32x32x16_bf16 v[160:175], v[220:223], v[136:139], v[160:175]\n\t" \
    "ds_read_b64_tr_b16 v[212:213], v247 offset:25088\n\t" \
    "ds_read_b64_tr_b16 v[214:215], v247 offset:25344\n\t" \
    "s_waitcnt lgkmcnt(4)\n\t" \
    "v_mfma_f32_32x32x16_bf16 v[144:159], v[224:227], v[140:143], v[144:159]\n\t" \
    "ds_read_b64_tr_b16 v[216:217], v247 offset:26624\n\t" \
    "ds_read_b64_tr_b16 v[218:219], v247 offset:26880\n\t" \
    "v_mfma_f32_32x32x16_bf16 v[160:175], v[228:231], v[140:143], v[160:175]\n\t" \
    "ds_read_b64_tr_b16 v[220:221], v247 offset:27136\n\t" \
    "ds_read_b64_tr_b16 v[222:223], v247 offset:27392\n\t" \
    "s_mov_b32 s68, s71\n\t" \
    "s_add_u32 s71, s71, 4096\n\t" \
    "s_mov_b32 s66, s69\n\t" \
    "v_add_u32 v248, 0x100, v248\n\t" \
    "s_branch .Lp1loop_%=\n\t" \
    ".Lp1end_%=:\n\t" \
    "s_waitcnt lgkmcnt(0)\n\t" \
    "s_barrier\n\t" \
    "s_nop 7\n\t" \
    "s_nop 7\n\t" \
    ""
#define P1_CLOB "v0", "v1", "v2", "v3", "v4", "v5", "v6", "v7", "v8", "v9", "v10", "v11", "v12", "v13", "v14", "v15", "v16", "v17", "v18", "v19", "v20", "v21", "v22", "v23", "v24", "v25", "v26", "v27", "v28", "v29", "v30", "v31", "v32", "v33", "v34", "v35", "v36", "v37", "v38", "v39", "v40", "v41", "v42", "v43", "v44", "v45", "v46", "v47", "v48", "v49", "v50", "v51", "v52", "v53", "v54", "v55", "v56", "v57", "v58", "v59", "v60", "v61", "v62", "v63", "v64", "v65", "v66", "v67", "v68", "v69", "v70", "v71", "v72", "v73", "v74", "v75", "v76", "v77", "v78", "v79", "v80", "v81", "v82", "v83", "v84", "v85", "v86", "v87", "v88", "v89", "v90", "v91", "v92", "v93", "v94", "v95", "v96", "v97", "v98", "v99", "v100", "v101", "v102", "v103", "v104", "v105", "v106", "v107", "v108", "v109", "v110", "v111", "v112", "v113", "v114", "v115", "v116", "v117", "v118", "v119", "v120", "v121", "v122", "v123", "v124", "v125", "v126", "v127", "v128", "v129", "v130", "v131", "v132", "v133", "v134", "v135", "v136", "v137", "v138", "v139", "v140", "v141", "v142", "v143", "v144", "v145", "v146", "v147", "v148", "v149", "v150", "v151", "v152", "v153", "v154", "v155", "v156", "v157", "v158", "v159", "v160", "v161", "v162", "v163", "v164", "v165", "v166", "v167", "v168", "v169", "v170", "v171", "v172", "v173", "v174", "v175", "v208", "v209", "v210", "v211", "v212", "v213", "v214", "v215", "v216", "v217", "v218", "v219", "v220", "v221", "v222", "v223", "v224", "v225", "v226", "v227", "v228", "v229", "v230", "v231", "v232", "v233", "v234", "v235", "v236", "v237", "v238", "v239", "v240", "v241", "v242", "v243", "v244", "v245"
#define K2_ALOOP \
    "s_nop 7\n\t" \
    "s_nop 7\n\t" \
    ".Lb1_%=:\n\t" \
    "s_waitcnt lgkmcnt(1)\n\t" \
    "v_mfma_f32_32x32x16_bf16 v[0:15], v[128:131], v[96:99], v[0:15]\n\t" \
    "v_exp_f32 v32, v32\n\t" \
    "v_exp_f32 v33, v33\n\t" \
    "v_exp_f32 v34, v34\n\t" \
    "v_mfma_f32_32x32x16_bf16 v[16:31], v[128:131], v[104:107], v[16:31]\n\t" \
    "v_exp_f32 v35, v35\n\t" \
    "v_exp_f32 v36, v36\n\t" \
    "v_exp_f32 v37, v37\n\t" \
    "v_exp_f32 v38, v38\n\t" \
    "v_exp_f32 v39, v39\n\t" \
    "v_exp_f32 v40, v40\n\t" \
    "v_mfma_f32_32x32x16_bf16 v[0:15], v[132:135], v[100:103], v[0:15]\n\t" \
    "v_exp_f32 v41, v41\n\t" \
    "v_cvt_pk_bf16_f32 v96, v32, v33\n\t" \
    "v_cvt_pk_bf16_f32 v97, v34, v35\n\t" \
    "v_exp_f32 v42, v42\n\t" \
    "v_mfma_f32_32x32x16_bf16 v[16:31], v[132:135], v[108:111], v[16:31]\n\t" \
    "v_exp_f32 v43, v43\n\t" \
    "v_exp_f32 v44, v44\n\t" \
    "v_exp_f32 v45, v45\n\t" \
    "v_cvt_pk_bf16_f32 v98, v36, v37\n\t" \
    "v_cvt_pk_bf16_f32 v99, v38, v39\n\t" \
    "v_exp_f32 v46, v46\n\t" \
    "v_exp_f32 v47, v47\n\t" \
    "s_waitcnt lgkmcnt(0)\n\t" \
    "v_mfma_f32_32x32x16_bf16 v[64:79], v[120:123], v[112:115], 0\n\t" \
    "ds_read_b128 v[128:131], v146 offset:0\n\t" \
    "v_exp_f32 v48, v48\n\t" \
    "v_exp_f32 v49, v49\n\t" \
    "v_cvt_pk_bf16_f32 v100, v40, v41\n\t" \
    "v_cvt_pk_bf16_f32 v101, v42, v43\n\t" \
    "v_exp_f32 v50, v50\n\t" \
    "v_exp_f32 v51, v51\n\t" \
    "v_exp_f32 v52, v52\n\t" \
    "v_mfma_f32_32x32x16_bf16 v[80:95], v[120:123], v[116:119], 0\n\t" \
    "ds_read_b128 v[132:135], v146 offset:640\n\t" \
    "v_exp_f32 v53, v53\n\t" \
    "v_cvt_pk_bf16_f32 v102, v44, v45\n\t" \
    "v_cvt_pk_bf16_f32 v103, v46, v47\n\t" \
    "v_exp_f32 v54, v54\n\t" \
    "v_exp_f32 v55, v55\n\t" \
    "v_exp_f32 v56, v56\n\t" \
    "v_exp_f32 v57, v57\n\t" \
    "v_mfma_f32_32x32x8_bf16 v[64:79], v[124:125], v[138:139], v[64:79]\n\t" \
    "ds_read_b128 v[120:123], v144 offset:2048\n\t" \
    "v_cvt_pk_bf16_f32 v104, v48, v49\n\t" \
    "v_cvt_pk_bf16_f32 v105, v50, v51\n\t" \
    "v_exp_f32 v58, v58\n\t" \
    "v_exp_f32 v59, v59\n\t" \
    "v_mfma_f32_32x32x8_bf16 v[80:95], v[124:125], v[142:143], v[80:95]\n\t" \
    "v_exp_f32 v60, v60\n\t" \
    "v_cvt_pk_bf16_f32 v106, v52, v53\n\t" \
    "v_cvt_pk_bf16_f32 v107, v54, v55\n\t" \
    "v_exp_f32 v61, v61\n\t" \
    "v_exp_f32 v62, v62\n\t" \
    "v_exp_f32 v63, v63\n\t" \
    "v_cvt_pk_bf16_f32 v108, v56, v57\n\t" \
    "v_cvt_pk_bf16_f32 v109, v58, v59\n\t" \
    "v_cvt_pk_bf16_f32 v110, v60, v61\n\t" \
    "v_cvt_pk_bf16_f32 v111, v62, v63\n\t" \
    "s_waitcnt lgkmcnt(1)\n\t" \
    "v_mfma_f32_32x32x16_bf16 v[0:15], v[128:131], v[96:99], v[0:15]\n\t" \
    "v_exp_f32 v64, v64\n\t" \
    "v_exp_f32 v65, v65\n\t" \
    "v_exp_f32 v66, v66\n\t" \
    "v_mfma_f32_32x32x16_bf16 v[16:31], v[128:131], v[104:107], v[16:31]\n\t" \
    "v_exp_f32 v67, v67\n\t" \
    "v_exp_f32 v68, v68\n\t" \
    "v_exp_f32 v69, v69\n\t" \
    "v_exp_f32 v70, v70\n\t" \
    "v_exp_f32 v71, v71\n\t" \
    "v_exp_f32 v72, v72\n\t" \
    "v_mfma_f32_32x32x16_bf16 v[0:15], v[132:135], v[100:103], v[0:15]\n\t" \
    "v_exp_f32 v73, v73\n\t" \
    "v_cvt_pk_bf16_f32 v96, v64, v65\n\t" \
    "v_cvt_pk_bf16_f32 v97, v66, v67\n\t" \
    "v_exp_f32 v74, v74\n\t" \
    "v_mfma_f32_32x32x16_bf16 v[16:31], v[132:135], v[108:111], v[16:31]\n\t" \
    "v_exp_f32 v75, v75\n\t" \
    "v_exp_f32 v76, v76\n\t" \
    "v_exp_f32 v77, v77\n\t" \
    "v_cvt_pk_bf16_f32 v98, v68, v69\n\t" \
    "v_cvt_pk_bf16_f32 v99, v70, v71\n\t" \
    "v_exp_f32 v78, v78\n\t" \
    "v_exp_f32 v79, v79\n\t" \
    "s_waitcnt lgkmcnt(0)\n\t" \
    "v_mfma_f32_32x32x16_bf16 v[32:47], v[120:123], v[112:115], 0\n\t" \
    "ds_read_b128 v[128:131], v146 offset:1280\n\t" \
    "v_exp_f32 v80, v80\n\t" \
    "v_exp_f32 v81, v81\n\t" \
    "v_cvt_pk_bf16_f32 v100, v72, v73\n\t" \
    "v_cvt_pk_bf16_f32 v101, v74, v75\n\t" \
    "v_exp_f32 v82, v82\n\t" \
    "v_exp_f32 v83, v83\n\t" \
    "v_exp_f32 v84, v84\n\t" \
    "v_mfma_f32_32x32x16_bf16 v[48:63], v[120:123], v[116:119], 0\n\t" \
    "ds_read_b128 v[132:135], v146 offset:1920\n\t" \
    "v_exp_f32 v85, v85\n\t" \
    "v_cvt_pk_bf16_f32 v102, v76, v77\n\t" \
    "v_cvt_pk_bf16_f32 v103, v78, v79\n\t" \
    "v_exp_f32 v86, v86\n\t" \
    "v_exp_f32 v87, v87\n\t" \
    "v_exp_f32 v88, v88\n\t" \
    "v_exp_f32 v89, v89\n\t" \
    "v_mfma_f32_32x32x8_bf16 v[32:47], v[126:127], v[136:137], v[32:47]\n\t" \
    "ds_read_b128 v[120:123], v144 offset:3072\n\t" \
    "v_cvt_pk_bf16_f32 v104, v80, v81\n\t" \
    "v_cvt_pk_bf16_f32 v105, v82, v83\n\t" \
    "v_exp_f32 v90, v90\n\t" \
    "v_exp_f32 v91, v91\n\t" \
    "v_mfma_f32_32x32x8_bf16 v[48:63], v[126:127], v[140:141], v[48:63]\n\t" \
    "v_exp_f32 v92, v92\n\t" \
    "v_cvt_pk_bf16_f32 v106, v84, v85\n\t" \
    "v_cvt_pk_bf16_f32 v107, v86, v87\n\t" \
    "v_exp_f32 v93, v93\n\t" \
    "v_exp_f32 v94, v94\n\t" \
    "v_exp_f32 v95, v95\n\t" \
    "v_cvt_pk_bf16_f32 v108, v88, v89\n\t" \
    "v_cvt_pk_bf16_f32 v109, v90, v91\n\t" \
    "v_cvt_pk_bf16_f32 v110, v92, v93\n\t" \
    "v_cvt_pk_bf16_f32 v111, v94, v95\n\t" \
    "s_waitcnt lgkmcnt(1)\n\t" \
    "v_mfma_f32_32x32x16_bf16 v[0:15], v[128:131], v[96:99], v[0:15]\n\t" \
    "v_exp_f32 v32, v32\n\t" \
    "v_exp_f32 v33, v33\n\t" \
    "v_exp_f32 v34, v34\n\t" \
    "v_mfma_f32_32x32x16_bf16 v[16:31], v[128:131], v[104:107], v[16:31]\n\t" \
    "v_exp_f32 v35, v35\n\t" \
    "v_exp_f32 v36, v36\n\t" \
    "v_exp_f32 v37, v37\n\t" \
    "v_exp_f32 v38, v38\n\t" \
    "v_exp_f32 v39, v39\n\t" \
    "v_exp_f32 v40, v40\n\t" \
    "v_mfma_f32_32x32x16_bf16 v[0:15], v[132:135], v[100:103], v[0:15]\n\t" \
    "v_exp_f32 v41, v41\n\t" \
    "v_cvt_pk_bf16_f32 v96, v32, v33\n\t" \
    "v_cvt_pk_bf16_f32 v97, v34, v35\n\t" \
    "v_exp_f32 v42, v42\n\t" \
    "v_mfma_f32_32x32x16_bf16 v[16:31], v[132:135], v[108:111], v[16:31]\n\t" \
    "v_exp_f32 v43, v43\n\t" \
    "v_exp_f32 v44, v44\n\t" \
    "v_exp_f32 v45, v45\n\t" \
    "v_cvt_pk_bf16_f32 v98, v36, v37\n\t" \
    "v_cvt_pk_bf16_f32 v99, v38, v39\n\t" \
    "v_exp_f32 v46, v46\n\t" \
    "v_exp_f32 v47, v47\n\t" \
    "s_waitcnt lgkmcnt(0)\n\t" \
    "v_mfma_f32_32x32x16_bf16 v[64:79], v[120:123], v[112:115], 0\n\t" \
    "ds_read_b128 v[128:131], v146 offset:2560\n\t" \
    "v_exp_f32 v48, v48\n\t" \
    "v_exp_f32 v49, v49\n\t" \
    "v_cvt_pk_bf16_f32 v100, v40, v41\n\t" \
    "v_cvt_pk_bf16_f32 v101, v42, v43\n\t" \
    "v_exp_f32 v50, v50\n\t" \
    "v_exp_f32 v51, v51\n\t" \
    "v_exp_f32 v52, v52\n\t" \
    "v_mfma_f32_32x32x16_bf16 v[80:95], v[120:123], v[116:119], 0\n\t" \
    "ds_read_b128 v[132:135], v146 offset:3200\n\t" \
    "v_exp_f32 v53, v53\n\t" \
    "v_cvt_pk_bf16_f32 v102, v44, v45\n\t" \
    "v_cvt_pk_bf16_f32 v103, v46, v47\n\t" \
    "v_exp_f32 v54, v54\n\t" \
    "v_exp_f32 v55, v55\n\t" \
    "v_exp_f32 v56, v56\n\t" \
    "v_exp_f32 v57, v57\n\t" \
    "v_mfma_f32_32x32x8_bf16 v[64:79], v[126:127], v[138:139], v[64:79]\n\t" \
    "ds_read_b128 v[120:123], v144 offset:9728\n\t" \
    "v_cvt_pk_bf16_f32 v104, v48, v49\n\t" \
    "v_cvt_pk_bf16_f32 v105, v50, v51\n\t" \
    "v_exp_f32 v58, v58\n\t" \
    "v_exp_f32 v59, v59\n\t" \
    "v_mfma_f32_32x32x8_bf16 v[80:95], v[126:127], v[142:143], v[80:95]\n\t" \
    "ds_read_b128 v[124:127], v145 offset:9728\n\t" \
    "v_exp_f32 v60, v60\n\t" \
    "v_cvt_pk_bf16_f32 v106, v52, v53\n\t" \
    "v_cvt_pk_bf16_f32 v107, v54, v55\n\t" \
    "v_exp_f32 v61, v61\n\t" \
    "v_exp_f32 v62, v62\n\t" \
    "v_exp_f32 v63, v63\n\t" \
    "v_cvt_pk_bf16_f32 v108, v56, v57\n\t" \
    "v_cvt_pk_bf16_f32 v109, v58, v59\n\t" \
    "v_cvt_pk_bf16_f32 v110, v60, v61\n\t" \
    "v_cvt_pk_bf16_f32 v111, v62, v63\n\t" \
    "s_waitcnt lgkmcnt(2)\n\t" \
    "v_mfma_f32_32x32x16_bf16 v[0:15], v[128:131], v[96:99], v[0:15]\n\t" \
    "v_exp_f32 v64, v64\n\t" \
    "v_exp_f32 v65, v65\n\t" \
    "v_exp_f32 v66, v66\n\t" \
    "v_mfma_f32_32x32x16_bf16 v[16:31], v[128:131], v[104:107], v[16:31]\n\t" \
    "v_exp_f32 v67, v67\n\t" \
    "v_exp_f32 v68, v68\n\t" \
    "v_exp_f32 v69, v69\n\t" \
    "v_exp_f32 v70, v70\n\t" \
    "v_exp_f32 v71, v71\n\t" \
    "v_exp_f32 v72, v72\n\t" \
    "v_mfma_f32_32x32x16_bf16 v[0:15], v[132:135], v[100:103], v[0:15]\n\t" \
    "v_exp_f32 v73, v73\n\t" \
    "v_cvt_pk_bf16_f32 v96, v64, v65\n\t" \
    "v_cvt_pk_bf16_f32 v97, v66, v67\n\t" \
    "v_exp_f32 v74, v74\n\t" \
    "v_mfma_f32_32x32x16_bf16 v[16:31], v[132:135], v[108:111], v[16:31]\n\t" \
    "v_exp_f32 v75, v75\n\t" \
    "v_exp_f32 v76, v76\n\t" \
    "v_exp_f32 v77, v77\n\t" \
    "v_cvt_pk_bf16_f32 v98, v68, v69\n\t" \
    "v_cvt_pk_bf16_f32 v99, v70, v71\n\t" \
    "v_exp_f32 v78, v78\n\t" \
    "v_exp_f32 v79, v79\n\t" \
    "s_waitcnt lgkmcnt(0)\n\t" \
    "v_mfma_f32_32x32x16_bf16 v[32:47], v[120:123], v[112:115], 0\n\t" \
    "ds_read_b128 v[128:131], v146 offset:3840\n\t" \
    "v_exp_f32 v80, v80\n\t" \
    "v_exp_f32 v81, v81\n\t" \
    "v_cvt_pk_bf16_f32 v100, v72, v73\n\t" \
    "v_cvt_pk_bf16_f32 v101, v74, v75\n\t" \
    "v_exp_f32 v82, v82\n\t" \
    "v_exp_f32 v83, v83\n\t" \
    "v_exp_f32 v84, v84\n\t" \
    "v_mfma_f32_32x32x16_bf16 v[48:63], v[120:123], v[116:119], 0\n\t" \
    "ds_read_b128 v[132:135], v146 offset:4480\n\t" \
    "v_exp_f32 v85, v85\n\t" \
    "v_cvt_pk_bf16_f32 v102, v76, v77\n\t" \
    "v_cvt_pk_bf16_f32 v103, v78, v79\n\t" \
    "v_exp_f32 v86, v86\n\t" \
    "v_exp_f32 v87, v87\n\t" \
    "v_exp_f32 v88, v88\n\t" \
    "v_exp_f32 v89, v89\n\t" \
    "v_mfma_f32_32x32x8_bf16 v[32:47], v[124:125], v[136:137], v[32:47]\n\t" \
    "ds_read_b128 v[120:123], v144 offset:10752\n\t" \
    "v_cvt_pk_bf16_f32 v104, v80, v81\n\t" \
    "v_cvt_pk_bf16_f32 v105, v82, v83\n\t" \
    "v_exp_f32 v90, v90\n\t" \
    "v_exp_f32 v91, v91\n\t" \
    "v_mfma_f32_32x32x8_bf16 v[48:63], v[124:125], v[140:141], v[48:63]\n\t" \
    "v_exp_f32 v92, v92\n\t" \
    "v_cvt_pk_bf16_f32 v106, v84, v85\n\t" \
    "v_cvt_pk_bf16_f32 v107, v86, v87\n\t" \
    "v_exp_f32 v93, v93\n\t" \
    "v_exp_f32 v94, v94\n\t" \
    "v_exp_f32 v95, v95\n\t" \
    "v_cvt_pk_bf16_f32 v108, v88, v89\n\t" \
    "v_cvt_pk_bf16_f32 v109, v90, v91\n\t" \
    "v_cvt_pk_bf16_f32 v110, v92, v93\n\t" \
    "v_cvt_pk_bf16_f32 v111, v94, v95\n\t" \
    "s_waitcnt lgkmcnt(1)\n\t" \
    "v_mfma_f32_32x32x16_bf16 v[0:15], v[128:131], v[96:99], v[0:15]\n\t" \
    "v_exp_f32 v32, v32\n\t" \
    "v_exp_f32 v33, v33\n\t" \
    "v_exp_f32 v34, v34\n\t" \
    "v_mfma_f32_32x32x16_bf16 v[16:31], v[128:131], v[104:107], v[16:31]\n\t" \
    "v_exp_f32 v35, v35\n\t" \
    "v_exp_f32 v36, v36\n\t" \
    "v_exp_f32 v37, v37\n\t" \
    "v_exp_f32 v38, v38\n\t" \
    "v_exp_f32 v39, v39\n\t" \
    "v_exp_f32 v40, v40\n\t" \
    "v_mfma_f32_32x32x16_bf16 v[0:15], v[132:135], v[100:103], v[0:15]\n\t" \
    "v_exp_f32 v41, v41\n\t" \
    "v_cvt_pk_bf16_f32 v96, v32, v33\n\t" \
    "v_cvt_pk_bf16_f32 v97, v34, v35\n\t" \
    "v_exp_f32 v42, v42\n\t" \
    "v_mfma_f32_32x32x16_bf16 v[16:31], v[132:135], v[108:111], v[16:31]\n\t" \
    "v_exp_f32 v43, v43\n\t" \
    "v_exp_f32 v44, v44\n\t" \
    "v_exp_f32 v45, v45\n\t" \
    "v_cvt_pk_bf16_f32 v98, v36, v37\n\t" \
    "v_cvt_pk_bf16_f32 v99, v38, v39\n\t" \
    "v_exp_f32 v46, v46\n\t" \
    "v_exp_f32 v47, v47\n\t" \
    "s_waitcnt lgkmcnt(0)\n\t" \
    "v_mfma_f32_32x32x16_bf16 v[64:79], v[120:123], v[112:115], 0\n\t" \
    "ds_read_b128 v[128:131], v146 offset:9728\n\t" \
    "v_exp_f32 v48, v48\n\t" \
    "v_exp_f32 v49, v49\n\t" \
    "v_cvt_pk_bf16_f32 v100, v40, v41\n\t" \
    "v_cvt_pk_bf16_f32 v101, v42, v43\n\t" \
    "v_exp_f32 v50, v50\n\t" \
    "v_exp_f32 v51, v51\n\t" \
    "v_exp_f32 v52, v52\n\t" \
    "v_mfma_f32_32x32x16_bf16 v[80:95], v[120:123], v[116:119], 0\n\t" \
    "ds_read_b128 v[132:135], v146 offset:10368\n\t" \
    "v_exp_f32 v53, v53\n\t" \
    "v_cvt_pk_bf16_f32 v102, v44, v45\n\t" \
    "v_cvt_pk_bf16_f32 v103, v46, v47\n\t" \
    "v_exp_f32 v54, v54\n\t" \
    "v_exp_f32 v55, v55\n\t" \
    "v_exp_f32 v56, v56\n\t" \
    "v_exp_f32 v57, v57\n\t" \
    "v_mfma_f32_32x32x8_bf16 v[64:79], v[124:125], v[138:139], v[64:79]\n\t" \
    "ds_read_b128 v[120:123], v144 offset:11776\n\t" \
    "v_cvt_pk_bf16_f32 v104, v48, v49\n\t" \
    "v_cvt_pk_bf16_f32 v105, v50, v51\n\t" \
    "v_exp_f32 v58, v58\n\t" \
    "v_exp_f32 v59, v59\n\t" \
    "v_mfma_f32_32x32x8_bf16 v[80:95], v[124:125], v[142:143], v[80:95]\n\t" \
    "ds_read_b32 v149, v148\n\t" \
    "v_exp_f32 v60, v60\n\t" \
    "v_cvt_pk_bf16_f32 v106, v52, v53\n\t" \
    "v_cvt_pk_bf16_f32 v107, v54, v55\n\t" \
    "v_exp_f32 v61, v61\n\t" \
    "v_exp_f32 v62, v62\n\t" \
    "v_exp_f32 v63, v63\n\t" \
    "v_cvt_pk_bf16_f32 v108, v56, v57\n\t" \
    "v_cvt_pk_bf16_f32 v109, v58, v59\n\t" \
    "v_cvt_pk_bf16_f32 v110, v60, v61\n\t" \
    "v_cvt_pk_bf16_f32 v111, v62, v63\n\t" \
    "s_waitcnt lgkmcnt(2)\n\t" \
    "v_mfma_f32_32x32x16_bf16 v[0:15], v[128:131], v[96:99], v[0:15]\n\t" \
    "v_exp_f32 v64, v64\n\t" \
    "v_exp_f32 v65, v65\n\t" \
    "v_exp_f32 v66, v66\n\t" \
    "v_mfma_f32_32x32x16_bf16 v[16:31], v[128:131], v[104:107], v[16:31]\n\t" \
    "v_exp_f32 v67, v67\n\t" \
    "v_exp_f32 v68, v68\n\t" \
    "v_exp_f32 v69, v69\n\t" \
    "v_exp_f32 v70, v70\n\t" \
    "v_exp_f32 v71, v71\n\t" \
    "v_exp_f32 v72, v72\n\t" \
    "s_and_b32 s42, %[t], 24\n\t" \
    "v_mfma_f32_32x32x16_bf16 v[0:15], v[132:135], v[100:103], v[0:15]\n\t" \
    "v_exp_f32 v73, v73\n\t" \
    "v_cvt_pk_bf16_f32 v96, v64, v65\n\t" \
    "v_cvt_pk_bf16_f32 v97, v66, v67\n\t" \
    "v_exp_f32 v74, v74\n\t" \
    "s_cmp_eq_u32 s42, 24\n\t" \
    "v_mfma_f32_32x32x16_bf16 v[16:31], v[132:135], v[108:111], v[16:31]\n\t" \
    "v_exp_f32 v75, v75\n\t" \
    "v_exp_f32 v76, v76\n\t" \
    "v_exp_f32 v77, v77\n\t" \
    "v_cvt_pk_bf16_f32 v98, v68, v69\n\t" \
    "v_cvt_pk_bf16_f32 v99, v70, v71\n\t" \
    "v_exp_f32 v78, v78\n\t" \
    "v_exp_f32 v79, v79\n\t" \
    "s_cselect_b32 s42, -77824, 0\n\t" \
    "s_waitcnt lgkmcnt(0)\n\t" \
    "v_mfma_f32_32x32x16_bf16 v[32:47], v[120:123], v[112:115], 0\n\t" \
    "ds_read_b128 v[128:131], v146 offset:11008\n\t" \
    "v_exp_f32 v80, v80\n\t" \
    "v_exp_f32 v81, v81\n\t" \
    "v_cvt_pk_bf16_f32 v100, v72, v73\n\t" \
    "v_cvt_pk_bf16_f32 v101, v74, v75\n\t" \
    "v_exp_f32 v82, v82\n\t" \
    "v_exp_f32 v83, v83\n\t" \
    "v_exp_f32 v84, v84\n\t" \
    "s_add_u32 s42, s42, 19456\n\t" \
    "v_mfma_f32_32x32x16_bf16 v[48:63], v[120:123], v[116:119], 0\n\t" \
    "ds_read_b128 v[132:135], v146 offset:11648\n\t" \
    "v_exp_f32 v85, v85\n\t" \
    "v_cvt_pk_bf16_f32 v102, v76, v77\n\t" \
    "v_cvt_pk_bf16_f32 v103, v78, v79\n\t" \
    "v_exp_f32 v86, v86\n\t" \
    "v_exp_f32 v87, v87\n\t" \
    "v_exp_f32 v88, v88\n\t" \
    "v_exp_f32 v89, v89\n\t" \
    "s_lshr_b32 s41, %[t], 1\n\t" \
    "v_mfma_f32_32x32x8_bf16 v[32:47], v[126:127], v[136:137], v[32:47]\n\t" \
    "ds_read_b128 v[120:123], v144 offset:12800\n\t" \
    "v_cvt_pk_bf16_f32 v104, v80, v81\n\t" \
    "v_cvt_pk_bf16_f32 v105, v82, v83\n\t" \
    "v_exp_f32 v90, v90\n\t" \
    "v_exp_f32 v91, v91\n\t" \
    "s_add_u32 s41, s41, 4\n\t" \
    "v_mfma_f32_32x32x8_bf16 v[48:63], v[126:127], v[140:141], v[48:63]\n\t" \
    "v_exp_f32 v92, v92\n\t" \
    "v_cvt_pk_bf16_f32 v106, v84, v85\n\t" \
    "v_cvt_pk_bf16_f32 v107, v86, v87\n\t" \
    "v_exp_f32 v93, v93\n\t" \
    "v_exp_f32 v94, v94\n\t" \
    "v_exp_f32 v95, v95\n\t" \
    "v_cvt_pk_bf16_f32 v108, v88, v89\n\t" \
    "v_cvt_pk_bf16_f32 v109, v90, v91\n\t" \
    "v_cvt_pk_bf16_f32 v110, v92, v93\n\t" \
    "v_cvt_pk_bf16_f32 v111, v94, v95\n\t" \
    "s_min_u32 s41, s41, 28\n\t" \
    "v_readfirstlane_b32 s40, v149\n\t" \
    "v_add_u32 v144, s42, v144\n\t" \
    "v_mad_i32_i24 v145, v147, s42, v145\n\t" \
    ".Lb3_%=:\n\t" \
    "s_lshr_b32 s40, s40, s41\n\t" \
    "s_and_b32 s40, s40, 15\n\t" \
    "s_cmp_eq_u32 s40, 4\n\t" \
    "s_cbranch_scc0 .Lb4_%=\n\t" \
    ".Lb6_%=:\n\t" \
    "s_waitcnt lgkmcnt(1)\n\t" \
    "v_mfma_f32_32x32x16_bf16 v[0:15], v[128:131], v[96:99], v[0:15]\n\t" \
    "v_exp_f32 v32, v32\n\t" \
    "v_exp_f32 v33, v33\n\t" \
    "v_exp_f32 v34, v34\n\t" \
    "v_mfma_f32_32x32x16_bf16 v[16:31], v[128:131], v[104:107], v[16:31]\n\t" \
    "v_exp_f32 v35, v35\n\t" \
    "v_exp_f32 v36, v36\n\t" \
    "v_exp_f32 v37, v37\n\t" \
    "v_exp_f32 v38, v38\n\t" \
    "v_exp_f32 v39, v39\n\t" \
    "v_exp_f32 v40, v40\n\t" \
    "v_mfma_f32_32x32x16_bf16 v[0:15], v[132:135], v[100:103], v[0:15]\n\t" \
    "v_exp_f32 v41, v41\n\t" \
    "v_cvt_pk_bf16_f32 v96, v32, v33\n\t" \
    "v_cvt_pk_bf16_f32 v97, v34, v35\n\t" \
    "v_exp_f32 v42, v42\n\t" \
    "v_mfma_f32_32x32x16_bf16 v[16:31], v[132:135], v[108:111], v[16:31]\n\t" \
    "v_exp_f32 v43, v43\n\t" \
    "v_exp_f32 v44, v44\n\t" \
    "v_exp_f32 v45, v45\n\t" \
    "v_cvt_pk_bf16_f32 v98, v36, v37\n\t" \
    "v_cvt_pk_bf16_f32 v99, v38, v39\n\t" \
    "v_exp_f32 v46, v46\n\t" \
    "v_exp_f32 v47, v47\n\t" \
    "s_waitcnt lgkmcnt(0)\n\t" \
    "v_mfma_f32_32x32x16_bf16 v[64:79], v[120:123], v[112:115], 0\n\t" \
    "ds_read_b128 v[128:131], v146 offset:12288\n\t" \
    "v_exp_f32 v48, v48\n\t" \
    "v_exp_f32 v49, v49\n\t" \
    "v_cvt_pk_bf16_f32 v100, v40, v41\n\t" \
    "v_cvt_pk_bf16_f32 v101, v42, v43\n\t" \
    "v_exp_f32 v50, v50\n\t" \
    "v_exp_f32 v51, v51\n\t" \
    "v_exp_f32 v52, v52\n\t" \
    "v_mfma_f32_32x32x16_bf16 v[80:95], v[120:123], v[116:119], 0\n\t" \
    "ds_read_b128 v[132:135], v146 offset:12928\n\t" \
    "v_exp_f32 v53, v53\n\t" \
    "v_cvt_pk_bf16_f32 v102, v44, v45\n\t" \
    "v_cvt_pk_bf16_f32 v103, v46, v47\n\t" \
    "v_exp_f32 v54, v54\n\t" \
    "v_exp_f32 v55, v55\n\t" \
    "v_exp_f32 v56, v56\n\t" \
    "v_exp_f32 v57, v57\n\t" \
    "v_mfma_f32_32x32x8_bf16 v[64:79], v[126:127], v[138:139], v[64:79]\n\t" \
    "ds_read_b128 v[120:123], v144 offset:0\n\t" \
    "v_cvt_pk_bf16_f32 v104, v48, v49\n\t" \
    "v_cvt_pk_bf16_f32 v105, v50, v51\n\t" \
    "v_exp_f32 v58, v58\n\t" \
    "v_exp_f32 v59, v59\n\t" \
    "v_mfma_f32_32x32x8_bf16 v[80:95], v[126:127], v[142:143], v[80:95]\n\t" \
    "ds_read_b128 v[124:127], v145\n\t" \
    "v_exp_f32 v60, v60\n\t" \
    "v_cvt_pk_bf16_f32 v106, v52, v53\n\t" \
    "v_cvt_pk_bf16_f32 v107, v54, v55\n\t" \
    "v_exp_f32 v61, v61\n\t" \
    "v_exp_f32 v62, v62\n\t" \
    "v_exp_f32 v63, v63\n\t" \
    "v_cvt_pk_bf16_f32 v108, v56, v57\n\t" \
    "v_cvt_pk_bf16_f32 v109, v58, v59\n\t" \
    "v_cvt_pk_bf16_f32 v110, v60, v61\n\t" \
    "v_cvt_pk_bf16_f32 v111, v62, v63\n\t" \
    "s_waitcnt lgkmcnt(2)\n\t" \
    "v_mfma_f32_32x32x16_bf16 v[0:15], v[128:131], v[96:99], v[0:15]\n\t" \
    "v_exp_f32 v64, v64\n\t" \
    "v_exp_f32 v65, v65\n\t" \
    "v_exp_f32 v66, v66\n\t" \
    "v_mfma_f32_32x32x16_bf16 v[16:31], v[128:131], v[104:107], v[16:31]\n\t" \
    "v_exp_f32 v67, v67\n\t" \
    "v_exp_f32 v68, v68\n\t" \
    "v_exp_f32 v69, v69\n\t" \
    "v_exp_f32 v70, v70\n\t" \
    "v_exp_f32 v71, v71\n\t" \
    "v_exp_f32 v72, v72\n\t" \
    "v_mfma_f32_32x32x16_bf16 v[0:15], v[132:135], v[100:103], v[0:15]\n\t" \
    "v_exp_f32 v73, v73\n\t" \
    "v_cvt_pk_bf16_f32 v96, v64, v65\n\t" \
    "v_cvt_pk_bf16_f32 v97, v66, v67\n\t" \
    "v_exp_f32 v74, v74\n\t" \
    "v_mfma_f32_32x32x16_bf16 v[16:31], v[132:135], v[108:111], v[16:31]\n\t" \
    "v_exp_f32 v75, v75\n\t" \
    "v_exp_f32 v76, v76\n\t" \
    "v_exp_f32 v77, v77\n\t" \
    "v_cvt_pk_bf16_f32 v98, v68, v69\n\t" \
    "v_cvt_pk_bf16_f32 v99, v70, v71\n\t" \
    "v_exp_f32 v78, v78\n\t" \
    "v_exp_f32 v79, v79\n\t" \
    "s_waitcnt lgkmcnt(0)\n\t" \
    "v_mfma_f32_32x32x16_bf16 v[32:47], v[120:123], v[112:115], 0\n\t" \
    "ds_read_b128 v[128:131], v146 offset:13568\n\t" \
    "v_exp_f32 v80, v80\n\t" \
    "v_exp_f32 v81, v81\n\t" \
    "v_cvt_pk_bf16_f32 v100, v72, v73\n\t" \
    "v_cvt_pk_bf16_f32 v101, v74, v75\n\t" \
    "v_exp_f32 v82, v82\n\t" \
    "v_exp_f32 v83, v83\n\t" \
    "v_exp_f32 v84, v84\n\t" \
    "v_mfma_f32_32x32x16_bf16 v[48:63], v[120:123], v[116:119], 0\n\t" \
    "ds_read_b128 v[132:135], v146 offset:14208\n\t" \
    "v_exp_f32 v85, v85\n\t" \
    "v_cvt_pk_bf16_f32 v102, v76, v77\n\t" \
    "v_cvt_pk_bf16_f32 v103, v78, v79\n\t" \
    "v_exp_f32 v86, v86\n\t" \
    "v_exp_f32 v87, v87\n\t" \
    "v_exp_f32 v88, v88\n\t" \
    "v_exp_f32 v89, v89\n\t" \
    "v_mfma_f32_32x32x8_bf16 v[32:47], v[124:125], v[136:137], v[32:47]\n\t" \
    "ds_read_b128 v[120:123], v144 offset:1024\n\t" \
    "v_cvt_pk_bf16_f32 v104, v80, v81\n\t" \
    "v_cvt_pk_bf16_f32 v105, v82, v83\n\t" \
    "v_exp_f32 v90, v90\n\t" \
    "v_exp_f32 v91, v91\n\t" \
    "v_mfma_f32_32x32x8_bf16 v[48:63], v[124:125], v[140:141], v[48:63]\n\t" \
    "v_exp_f32 v92, v92\n\t" \
    "v_cvt_pk_bf16_f32 v106, v84, v85\n\t" \
    "v_cvt_pk_bf16_f32 v107, v86, v87\n\t" \
    "v_exp_f32 v93, v93\n\t" \
    "v_exp_f32 v94, v94\n\t" \
    "v_exp_f32 v95, v95\n\t" \
    "v_cvt_pk_bf16_f32 v108, v88, v89\n\t" \
    "v_cvt_pk_bf16_f32 v109, v90, v91\n\t" \
    "v_cvt_pk_bf16_f32 v110, v92, v93\n\t" \
    "v_cvt_pk_bf16_f32 v111, v94, v95\n\t" \
    "ds_add_u32 v150, v151\n\t" \
    "s_add_u32 %[t], %[t], 8\n\t" \
    "v_max_f32 v153, v10, v26\n\t" \
    "v_add_u32 v146, s42, v146\n\t" \
    "v_cmp_lt_f32 vcc, %[trig], v153\n\t" \
    "v_lshlrev_b32 v151, 4, v151\n\t" \
    "s_cmp_ge_u32 %[t], 64\n\t" \
    "s_cbranch_scc1 .Lb2_%=\n\t" \
    "s_cbranch_vccz .Lb1_%=\n\t" \
    "s_branch .Lb2_%=\n\t" \
    ".Lb4_%=:\n\t" \
    "s_sub_u32 %[spin], %[spin], 1\n\t" \
    "s_cmp_lt_i32 %[spin], 0\n\t" \
    "s_cbranch_scc1 .Lb6_%=\n\t" \
    "s_sleep 4\n\t" \
    "ds_read_b32 v149, v148\n\t" \
    "s_waitcnt lgkmcnt(0)\n\t" \
    "v_readfirstlane_b32 s40, v149\n\t" \
    "s_branch .Lb3_%=\n\t" \
    ".Lb2_%=:\n\t" \
    "s_waitcnt lgkmcnt(0)\n\t" \
    "s_nop 7\n\t" \
    "s_nop 7\n\t" \
    ""
#define K2_GLOOP \
    "s_waitcnt vmcnt(18)\n\t" \
    "s_barrier\n\t" \
    "s_mul_i32 s66, s64, 19456\n\t" \
    "s_and_b32 s67, s64, 3\n\t" \
    "s_mul_i32 s67, s67, 19456\n\t" \
    "s_add_u32 s67, s67, s71\n\t" \
    "s_add_u32 m0, s67, 0\n\t" \
    "s_add_u32 s65, s66, 0\n\t" \
    "buffer_load_dwordx4 v153, s[52:55], s65 offen lds\n\t" \
    "s_add_u32 m0, s67, 4096\n\t" \
    "s_add_u32 s65, s66, 4096\n\t" \
    "buffer_load_dwordx4 v153, s[52:55], s65 offen lds\n\t" \
    "s_add_u32 m0, s67, 8192\n\t" \
    "s_add_u32 s65, s66, 8192\n\t" \
    "buffer_load_dwordx4 v153, s[52:55], s65 offen lds\n\t" \
    "s_add_u32 m0, s67, 12288\n\t" \
    "s_add_u32 s65, s66, 12288\n\t" \
    "buffer_load_dwordx4 v153, s[52:55], s65 offen lds\n\t" \
    "s_add_u32 m0, s67, s70\n\t" \
    "s_add_u32 s65, s66, s70\n\t" \
    "buffer_load_dwordx4 v153, s[52:55], s65 offen lds\n\t" \
    "s_add_u32 s64, s64, 1\n\t" \
    "s_add_u32 s74, s74, 4\n\t" \
    "s_cmp_eq_u32 s64, 8\n\t" \
    "s_cselect_b32 s75, 16, 8\n\t" \
    "s_waitcnt vmcnt(17)\n\t" \
    "s_barrier\n\t" \
    "s_mul_i32 s66, s64, 19456\n\t" \
    "s_and_b32 s67, s64, 3\n\t" \
    "s_mul_i32 s67, s67, 19456\n\t" \
    "s_add_u32 s67, s67, s71\n\t" \
    "s_add_u32 m0, s67, 0\n\t" \
    "s_add_u32 s65, s66, 0\n\t" \
    "buffer_load_dwordx4 v153, s[52:55], s65 offen lds\n\t" \
    "s_add_u32 m0, s67, 4096\n\t" \
    "s_add_u32 s65, s66, 4096\n\t" \
    "buffer_load_dwordx4 v153, s[52:55], s65 offen lds\n\t" \
    "s_add_u32 m0, s67, 8192\n\t" \
    "s_add_u32 s65, s66, 8192\n\t" \
    "buffer_load_dwordx4 v153, s[52:55], s65 offen lds\n\t" \
    "s_add_u32 m0, s67, 12288\n\t" \
    "s_add_u32 s65, s66, 12288\n\t" \
    "buffer_load_dwordx4 v153, s[52:55], s65 offen lds\n\t" \
    "s_add_u32 m0, s67, s70\n\t" \
    "s_add_u32 s65, s66, s70\n\t" \
    "buffer_load_dwordx4 v153, s[52:55], s65 offen lds\n\t" \
    "s_add_u32 s64, s64, 1\n\t" \
    "s_add_u32 s74, s74, 4\n\t" \
    "s_cmp_eq_u32 s64, 8\n\t" \
    "s_cselect_b32 s75, 16, 8\n\t" \
    "s_waitcnt vmcnt(16)\n\t" \
    "s_barrier\n\t" \
    "s_mul_i32 s66, s64, 19456\n\t" \
    "s_and_b32 s67, s64, 3\n\t" \
    "s_mul_i32 s67, s67, 19456\n\t" \
    "s_add_u32 s67, s67, s71\n\t" \
    "s_add_u32 m0, s67, 0\n\t" \
    "s_add_u32 s65, s66, 0\n\t" \
    "buffer_load_dwordx4 v153, s[52:55], s65 offen lds\n\t" \
    "s_add_u32 m0, s67, 4096\n\t" \
    "s_add_u32 s65, s66, 4096\n\t" \
    "buffer_load_dwordx4 v153, s[52:55], s65 offen lds\n\t" \
    "s_add_u32 m0, s67, 8192\n\t" \
    "s_add_u32 s65, s66, 8192\n\t" \
    "buffer_load_dwordx4 v153, s[52:55], s65 offen lds\n\t" \
    "s_add_u32 m0, s67, 12288\n\t" \
    "s_add_u32 s65, s66, 12288\n\t" \
    "buffer_load_dwordx4 v153, s[52:55], s65 offen lds\n\t" \
    "s_add_u32 m0, s67, s70\n\t" \
    "s_add_u32 s65, s66, s70\n\t" \
    "buffer_load_dwordx4 v153, s[52:55], s65 offen lds\n\t" \
    "s_add_u32 s64, s64, 1\n\t" \
    "s_add_u32 s74, s74, 4\n\t" \
    "s_cmp_eq_u32 s64, 8\n\t" \
    "s_cselect_b32 s75, 16, 8\n\t" \
    "s_waitcnt vmcnt(15)\n\t" \
    "s_barrier\n\t" \
    "buffer_load_dword v171, v170, s[88:91], s82 offen\n\t" \
    "s_add_u32 s82, s82, 147456\n\t" \
    "buffer_load_dword v171, v170, s[88:91], s82 offen\n\t" \
    "s_add_u32 s82, s82, 147456\n\t" \
    "buffer_load_dword v171, v170, s[88:91], s82 offen\n\t" \
    "s_add_u32 s82, s82, 147456\n\t" \
    "buffer_load_dword v171, v170, s[88:91], s82 offen\n\t" \
    "s_add_u32 s82, s82, 147456\n\t" \
    "v_mov_b32 v0, 0\n\t" \
    "v_mov_b32 v1, 0\n\t" \
    "v_mov_b32 v2, 0\n\t" \
    "v_mov_b32 v3, 0\n\t" \
    "v_mov_b32 v4, 0\n\t" \
    "v_mov_b32 v5, 0\n\t" \
    "v_mov_b32 v6, 0\n\t" \
    "v_mov_b32 v7, 0\n\t" \
    "v_mov_b32 v8, 0\n\t" \
    "v_mov_b32 v9, 0\n\t" \
    "v_mov_b32 v10, 0\n\t" \
    "v_mov_b32 v11, 0\n\t" \
    "v_mov_b32 v12, 0\n\t" \
    "v_mov_b32 v13, 0\n\t" \
    "v_mov_b32 v14, 0\n\t" \
    "v_mov_b32 v15, 0\n\t" \
    "v_mov_b32 v16, 0\n\t" \
    "v_mov_b32 v17, 0\n\t" \
    "v_mov_b32 v18, 0\n\t" \
    "v_mov_b32 v19, 0\n\t" \
    "v_mov_b32 v20, 0\n\t" \
    "v_mov_b32 v21, 0\n\t" \
    "v_mov_b32 v22, 0\n\t" \
    "v_mov_b32 v23, 0\n\t" \
    "v_mov_b32 v24, 0\n\t" \
    "v_mov_b32 v25, 0\n\t" \
    "v_mov_b32 v26, 0\n\t" \
    "v_mov_b32 v27, 0\n\t" \
    "v_mov_b32 v28, 0\n\t" \
    "v_mov_b32 v29, 0\n\t" \
    "v_mov_b32 v30, 0\n\t" \
    "v_mov_b32 v31, 0\n\t" \
    "v_mov_b32 v32, 0\n\t" \
    "v_mov_b32 v33, 0\n\t" \
    "v_mov_b32 v34, 0\n\t" \
    "v_mov_b32 v35, 0\n\t" \
    "v_mov_b32 v36, 0\n\t" \
    "v_mov_b32 v37, 0\n\t" \
    "v_mov_b32 v38, 0\n\t" \
    "v_mov_b32 v39, 0\n\t" \
    "v_mov_b32 v40, 0\n\t" \
    "v_mov_b32 v41, 0\n\t" \
    "v_mov_b32 v42, 0\n\t" \
    "v_mov_b32 v43, 0\n\t" \
    "v_mov_b32 v44, 0\n\t" \
    "v_mov_b32 v45, 0\n\t" \
    "v_mov_b32 v46, 0\n\t" \
    "v_mov_b32 v47, 0\n\t" \
    "v_mov_b32 v48, 0\n\t" \
    "v_mov_b32 v49, 0\n\t" \
    "v_mov_b32 v50, 0\n\t" \
    "v_mov_b32 v51, 0\n\t" \
    "v_mov_b32 v52, 0\n\t" \
    "v_mov_b32 v53, 0\n\t" \
    "v_mov_b32 v54, 0\n\t" \
    "v_mov_b32 v55, 0\n\t" \
    "v_mov_b32 v56, 0\n\t" \
    "v_mov_b32 v57, 0\n\t" \
    "v_mov_b32 v58, 0\n\t" \
    "v_mov_b32 v59, 0\n\t" \
    "v_mov_b32 v60, 0\n\t" \
    "v_mov_b32 v61, 0\n\t" \
    "v_mov_b32 v62, 0\n\t" \
    "v_mov_b32 v63, 0\n\t" \
    "v_mov_b32 v64, 0\n\t" \
    "v_mov_b32 v65, 0\n\t" \
    "v_mov_b32 v66, 0\n\t" \
    "v_mov_b32 v67, 0\n\t" \
    "v_mov_b32 v68, 0\n\t" \
    "v_mov_b32 v69, 0\n\t" \
    "v_mov_b32 v70, 0\n\t" \
    "v_mov_b32 v71, 0\n\t" \
    "v_mov_b32 v72, 0\n\t" \
    "v_mov_b32 v73, 0\n\t" \
    "v_mov_b32 v74, 0\n\t" \
    "v_mov_b32 v75, 0\n\t" \
    "v_mov_b32 v76, 0\n\t" \
    "v_mov_b32 v77, 0\n\t" \
    "v_mov_b32 v78, 0\n\t" \
    "v_mov_b32 v79, 0\n\t" \
    "v_mov_b32 v169, 0\n\t" \
    "s_mov_b32 s65, 0\n\t" \
    "s_waitcnt vmcnt(4)\n\t" \
    "s_barrier\n\t" \
    "s_setprio 3\n\t" \
    "s_cmp_lg_u32 s83, 0\n\t" \
    "s_cbranch_scc1 .Lgx_%=\n\t" \
    "s_mov_b64 exec, 1\n\t" \
    "global_atomic_add v169, v155, s[72:73]\n\t" \
    "s_mov_b64 exec, -1\n\t" \
    ".Lgx_%=:\n\t" \
    "s_barrier\n\t" \
    "s_add_u32 m0, s61, 0\n\t" \
    "s_nop 0\n\t" \
    "buffer_load_dwordx4 v159, s[40:43], s56 offen lds\n\t" \
    "s_add_u32 m0, s61, 1024\n\t" \
    "s_add_u32 s56, s56, s78\n\t" \
    "buffer_load_dwordx4 v159, s[40:43], s57 offen lds\n\t" \
    "s_add_u32 m0, m0, s69\n\t" \
    "s_add_u32 s57, s57, s78\n\t" \
    "s_mov_b64 exec, 0xffff\n\t" \
    "buffer_load_dwordx4 v158, s[44:47], s58 offen lds\n\t" \
    "s_mov_b64 exec, -1\n\t" \
    "s_add_u32 s58, s58, s78\n\t" \
    "s_add_u32 m0, s61, 9216\n\t" \
    "s_nop 0\n\t" \
    "buffer_load_dwordx4 v159, s[40:43], s56 offen lds\n\t" \
    "s_add_u32 m0, s61, 10240\n\t" \
    "s_add_u32 s56, s56, s78\n\t" \
    "buffer_load_dwordx4 v159, s[40:43], s57 offen lds\n\t" \
    "s_add_u32 m0, m0, s69\n\t" \
    "s_add_u32 s57, s57, s78\n\t" \
    "s_mov_b64 exec, 0xffff\n\t" \
    "buffer_load_dwordx4 v158, s[44:47], s58 offen lds\n\t" \
    "s_mov_b64 exec, -1\n\t" \
    "s_add_u32 s58, s58, s78\n\t" \
    "s_add_u32 m0, s61, 18432\n\t" \
    "s_nop 0\n\t" \
    "buffer_load_dwordx4 v159, s[40:43], s56 offen lds\n\t" \
    "s_add_u32 m0, s61, 19456\n\t" \
    "s_add_u32 s56, s56, s78\n\t" \
    "buffer_load_dwordx4 v159, s[40:43], s57 offen lds\n\t" \
    "s_add_u32 m0, m0, s69\n\t" \
    "s_add_u32 s57, s57, s78\n\t" \
    "s_mov_b64 exec, 0xffff\n\t" \
    "buffer_load_dwordx4 v158, s[44:47], s58 offen lds\n\t" \
    "s_mov_b64 exec, -1\n\t" \
    "s_add_u32 s58, s58, s78\n\t" \
    "s_add_u32 m0, s61, 27648\n\t" \
    "s_nop 0\n\t" \
    "buffer_load_dwordx4 v159, s[40:43], s56 offen lds\n\t" \
    "s_add_u32 m0, s61, 28672\n\t" \
    "s_add_u32 s56, s56, s78\n\t" \
    "buffer_load_dwordx4 v159, s[40:43], s57 offen lds\n\t" \
    "s_add_u32 m0, m0, s69\n\t" \
    "s_add_u32 s57, s57, s78\n\t" \
    "s_mov_b64 exec, 0xffff\n\t" \
    "buffer_load_dwordx4 v158, s[44:47], s58 offen lds\n\t" \
    "s_mov_b64 exec, -1\n\t" \
    "s_add_u32 s58, s58, s78\n\t" \
    "s_add_u32 m0, s62, 0\n\t" \
    "s_nop 0\n\t" \
    "buffer_load_dwordx4 v159, s[40:43], s56 offen lds\n\t" \
    "s_add_u32 m0, s62, 1024\n\t" \
    "s_add_u32 s56, s56, s78\n\t" \
    "buffer_load_dwordx4 v159, s[40:43], s57 offen lds\n\t" \
    "s_add_u32 m0, m0, s69\n\t" \
    "s_add_u32 s57, s57, s78\n\t" \
    "s_mov_b64 exec, 0xffff\n\t" \
    "buffer_load_dwordx4 v158, s[44:47], s58 offen lds\n\t" \
    "s_mov_b64 exec, -1\n\t" \
    "s_add_u32 s58, s58, s78\n\t" \
    "s_add_u32 m0, s62, 9216\n\t" \
    "s_nop 0\n\t" \
    "buffer_load_dwordx4 v159, s[40:43], s56 offen lds\n\t" \
    "s_add_u32 m0, s62, 10240\n\t" \
    "s_add_u32 s56, s56, s78\n\t" \
    "buffer_load_dwordx4 v159, s[40:43], s57 offen lds\n\t" \
    "s_add_u32 m0, m0, s69\n\t" \
    "s_add_u32 s57, s57, s78\n\t" \
    "s_mov_b64 exec, 0xffff\n\t" \
    "buffer_load_dwordx4 v158, s[44:47], s58 offen lds\n\t" \
    "s_mov_b64 exec, -1\n\t" \
    "s_add_u32 s58, s58, s78\n\t" \
    ".Lha_%=:\n\t" \
    "global_load_dword v168, v169, s[72:73] offset:0 sc1\n\t" \
    "s_waitcnt vmcnt(0)\n\t" \
    "v_readfirstlane_b32 s66, v168\n\t" \
    "s_cmp_ge_u32 s66, 4\n\t" \
    "s_cbranch_scc1 .Lia_%=\n\t" \
    "s_sub_u32 s77, s77, 1\n\t" \
    "s_cmp_lt_i32 s77, 0\n\t" \
    "s_cbranch_scc1 .Lia_%=\n\t" \
    "s_sleep 2\n\t" \
    "s_branch .Lha_%=\n\t" \
    ".Lia_%=:\n\t" \
    "buffer_load_dwordx4 v[120:123], v153, s[48:51], s59 offen sc1\n\t" \
    "s_add_u32 s59, s59, 2048\n\t" \
    "buffer_load_dwordx4 v[124:127], v153, s[48:51], s60 offen sc1\n\t" \
    "s_add_u32 s60, s60, 2048\n\t" \
    "buffer_load_dwordx4 v[128:131], v153, s[48:51], s59 offen sc1\n\t" \
    "s_add_u32 s59, s59, 2048\n\t" \
    "buffer_load_dwordx4 v[132:135], v153, s[48:51], s60 offen sc1\n\t" \
    "s_add_u32 s60, s60, 2048\n\t" \
    "buffer_load_dwordx4 v[136:139], v153, s[48:51], s59 offen sc1\n\t" \
    "s_add_u32 s59, s59, 2048\n\t" \
    "buffer_load_dwordx4 v[140:143], v153, s[48:51], s60 offen sc1\n\t" \
    "s_add_u32 s60, s60, 2048\n\t" \
    "buffer_load_dwordx4 v[144:147], v153, s[48:51], s59 offen sc1\n\t" \
    "s_add_u32 s59, s59, 2048\n\t" \
    "buffer_load_dwordx4 v[148:151], v153, s[48:51], s60 offen sc1\n\t" \
    "s_add_u32 s60, s60, 2048\n\t" \
    "ds_add_u32 v162, v155\n\t" \
    ".Lgp_%=:\n\t" \
    "ds_read_b128 v[164:167], v154\n\t" \
    "s_waitcnt lgkmcnt(0)\n\t" \
    "v_readfirstlane_b32 s66, v167\n\t" \
    "s_cmp_ge_u32 s66, 4\n\t" \
    "s_cbranch_scc1 .Lgq_%=\n\t" \
    "s_sub_u32 s77, s77, 1\n\t" \
    "s_cmp_lt_i32 s77, 0\n\t" \
    "s_cbranch_scc1 .Lgq_%=\n\t" \
    "s_sleep 1\n\t" \
    "s_branch .Lgp_%=\n\t" \
    ".Lgq_%=:\n\t" \
    "ds_read_b64_tr_b16 v[80:81], v152 offset:0\n\t" \
    "ds_read_b64_tr_b16 v[82:83], v152 offset:512\n\t" \
    "ds_read_b64_tr_b16 v[84:85], v152 offset:2048\n\t" \
    "ds_read_b64_tr_b16 v[86:87], v152 offset:2560\n\t" \
    "ds_read_b64_tr_b16 v[88:89], v152 offset:4096\n\t" \
    "ds_read_b64_tr_b16 v[90:91], v152 offset:4608\n\t" \
    "ds_read_b64_tr_b16 v[92:93], v152 offset:6144\n\t" \
    "ds_read_b64_tr_b16 v[94:95], v152 offset:6656\n\t" \
    "ds_read_b64_tr_b16 v[96:97], v152 offset:8192\n\t" \
    "ds_read_b64_tr_b16 v[98:99], v152 offset:8704\n\t" \
    "s_waitcnt vmcnt(0)\n\t" \
    "s_setprio 0\n\t" \
    ".Lg1_%=:\n\t" \
    "s_waitcnt vmcnt(15) lgkmcnt(0)\n\t" \
    "v_mfma_f32_32x32x16_bf16 v[0:15], v[80:83], v[120:123], v[0:15]\n\t" \
    "ds_add_u32 v161, v155\n\t" \
    "ds_read_b128 v[164:167], v154\n\t" \
    "ds_read_b64_tr_b16 v[100:101], v152 offset:1024\n\t" \
    "ds_read_b64_tr_b16 v[102:103], v152 offset:1536\n\t" \
    "v_mfma_f32_32x32x16_bf16 v[16:31], v[84:87], v[120:123], v[16:31]\n\t" \
    "ds_read_b64_tr_b16 v[104:105], v152 offset:3072\n\t" \
    "ds_read_b64_tr_b16 v[106:107], v152 offset:3584\n\t" \
    "ds_read_b64_tr_b16 v[108:109], v152 offset:5120\n\t" \
    "ds_read_b64_tr_b16 v[110:111], v152 offset:5632\n\t" \
    "ds_read_b64_tr_b16 v[112:113], v152 offset:7168\n\t" \
    "ds_read_b64_tr_b16 v[114:115], v152 offset:7680\n\t" \
    "v_mfma_f32_32x32x16_bf16 v[32:47], v[88:91], v[120:123], v[32:47]\n\t" \
    "s_setprio 3\n\t" \
    "s_waitcnt lgkmcnt(8)\n\t" \
    "v_readfirstlane_b32 s66, v167\n\t" \
    "v_readfirstlane_b32 s67, v165\n\t" \
    ".Lga0_%=:\n\t" \
    "s_cmp_ge_u32 s66, s63\n\t" \
    "s_cbranch_scc0 .Lgw0_%=\n\t" \
    ".Lgc0_%=:\n\t" \
    "s_setprio 0\n\t" \
    "v_mfma_f32_32x32x16_bf16 v[48:63], v[92:95], v[120:123], v[48:63]\n\t" \
    "s_add_u32 m0, s62, 18432\n\t" \
    "s_nop 0\n\t" \
    "buffer_load_dwordx4 v159, s[40:43], s56 offen lds\n\t" \
    "s_add_u32 m0, s62, 19456\n\t" \
    "s_add_u32 s56, s56, s78\n\t" \
    "buffer_load_dwordx4 v159, s[40:43], s57 offen lds\n\t" \
    "v_mfma_f32_32x32x16_bf16 v[64:79], v[96:99], v[120:123], v[64:79]\n\t" \
    "s_add_u32 m0, m0, s69\n\t" \
    "s_add_u32 s57, s57, s78\n\t" \
    "s_mov_b64 exec, 0xffff\n\t" \
    "buffer_load_dwordx4 v158, s[44:47], s58 offen lds\n\t" \
    "s_mov_b64 exec, -1\n\t" \
    "s_add_u32 s58, s58, s78\n\t" \
    "buffer_load_dwordx4 v[120:123], v153, s[48:51], s59 offen sc1\n\t" \
    "s_add_u32 s59, s59, 2048\n\t" \
    "s_waitcnt lgkmcnt(0)\n\t" \
    "v_mfma_f32_32x32x16_bf16 v[0:15], v[100:103], v[124:127], v[0:15]\n\t" \
    "ds_read_b64_tr_b16 v[80:81], v152 offset:9216\n\t" \
    "ds_read_b64_tr_b16 v[82:83], v152 offset:9728\n\t" \
    "ds_read_b64_tr_b16 v[84:85], v152 offset:11264\n\t" \
    "ds_read_b64_tr_b16 v[86:87], v152 offset:11776\n\t" \
    "v_mfma_f32_32x32x16_bf16 v[16:31], v[104:107], v[124:127], v[16:31]\n\t" \
    "ds_read_b64_tr_b16 v[88:89], v152 offset:13312\n\t" \
    "ds_read_b64_tr_b16 v[90:91], v152 offset:13824\n\t" \
    "ds_read_b64_tr_b16 v[92:93], v152 offset:15360\n\t" \
    "ds_read_b64_tr_b16 v[94:95], v152 offset:15872\n\t" \
    "ds_read_b64_tr_b16 v[96:97], v152 offset:17408\n\t" \
    "ds_read_b64_tr_b16 v[98:99], v152 offset:17920\n\t" \
    "v_mfma_f32_32x32x16_bf16 v[32:47], v[108:111], v[124:127], v[32:47]\n\t" \
    "s_lshr_b32 s67, s67, s74\n\t" \
    "s_and_b32 s67, s67, 15\n\t" \
    "s_cmp_eq_u32 s67, s75\n\t" \
    "s_cbranch_scc1 .Lgk1_%=\n\t" \
    ".Lgk2_%=:\n\t" \
    "v_mfma_f32_32x32x16_bf16 v[48:63], v[112:115], v[124:127], v[48:63]\n\t" \
    "buffer_load_dwordx4 v[124:127], v153, s[48:51], s60 offen sc1\n\t" \
    "s_add_u32 s60, s60, 2048\n\t" \
    "s_waitcnt vmcnt(15) lgkmcnt(0)\n\t" \
    "v_mfma_f32_32x32x16_bf16 v[0:15], v[80:83], v[128:131], v[0:15]\n\t" \
    "ds_add_u32 v162, v155\n\t" \
    "ds_read_b128 v[164:167], v154\n\t" \
    "ds_read_b64_tr_b16 v[100:101], v152 offset:10240\n\t" \
    "ds_read_b64_tr_b16 v[102:103], v152 offset:10752\n\t" \
    "v_mfma_f32_32x32x16_bf16 v[16:31], v[84:87], v[128:131], v[16:31]\n\t" \
    "ds_read_b64_tr_b16 v[104:105], v152 offset:12288\n\t" \
    "ds_read_b64_tr_b16 v[106:107], v152 offset:12800\n\t" \
    "ds_read_b64_tr_b16 v[108:109], v152 offset:14336\n\t" \
    "ds_read_b64_tr_b16 v[110:111], v152 offset:14848\n\t" \
    "ds_read_b64_tr_b16 v[112:113], v152 offset:16384\n\t" \
    "ds_read_b64_tr_b16 v[114:115], v152 offset:16896\n\t" \
    "v_mfma_f32_32x32x16_bf16 v[32:47], v[88:91], v[128:131], v[32:47]\n\t" \
    "s_setprio 3\n\t" \
    "s_waitcnt lgkmcnt(8)\n\t" \
    "v_readfirstlane_b32 s66, v164\n\t" \
    ".Lga1_%=:\n\t" \
    "s_cmp_ge_u32 s66, s63\n\t" \
    "s_cbranch_scc0 .Lgw1_%=\n\t" \
    ".Lgc1_%=:\n\t" \
    "s_setprio 0\n\t" \
    "v_mfma_f32_32x32x16_bf16 v[48:63], v[92:95], v[128:131], v[48:63]\n\t" \
    "s_add_u32 m0, s62, 27648\n\t" \
    "s_nop 0\n\t" \
    "buffer_load_dwordx4 v159, s[40:43], s56 offen lds\n\t" \
    "s_add_u32 m0, s62, 28672\n\t" \
    "s_add_u32 s56, s56, s78\n\t" \
    "buffer_load_dwordx4 v159, s[40:43], s57 offen lds\n\t" \
    "v_mfma_f32_32x32x16_bf16 v[64:79], v[96:99], v[128:131], v[64:79]\n\t" \
    "s_add_u32 m0, m0, s69\n\t" \
    "s_add_u32 s57, s57, s78\n\t" \
    "s_mov_b64 exec, 0xffff\n\t" \
    "buffer_load_dwordx4 v158, s[44:47], s58 offen lds\n\t" \
    "s_mov_b64 exec, -1\n\t" \
    "s_add_u32 s58, s58, s78\n\t" \
    "buffer_load_dwordx4 v[128:131], v153, s[48:51], s59 offen sc1\n\t" \
    "s_add_u32 s59, s59, 2048\n\t" \
    "s_waitcnt lgkmcnt(0)\n\t" \
    "v_mfma_f32_32x32x16_bf16 v[0:15], v[100:103], v[132:135], v[0:15]\n\t" \
    "ds_read_b64_tr_b16 v[80:81], v152 offset:18432\n\t" \
    "ds_read_b64_tr_b16 v[82:83], v152 offset:18944\n\t" \
    "ds_read_b64_tr_b16 v[84:85], v152 offset:20480\n\t" \
    "ds_read_b64_tr_b16 v[86:87], v152 offset:20992\n\t" \
    "v_mfma_f32_32x32x16_bf16 v[16:31], v[104:107], v[132:135], v[16:31]\n\t" \
    "ds_read_b64_tr_b16 v[88:89], v152 offset:22528\n\t" \
    "ds_read_b64_tr_b16 v[90:91], v152 offset:23040\n\t" \
    "ds_read_b64_tr_b16 v[92:93], v152 offset:24576\n\t" \
    "ds_read_b64_tr_b16 v[94:95], v152 offset:25088\n\t" \
    "ds_read_b64_tr_b16 v[96:97], v152 offset:26624\n\t" \
    "ds_read_b64_tr_b16 v[98:99], v152 offset:27136\n\t" \
    "v_mfma_f32_32x32x16_bf16 v[32:47], v[108:111], v[132:135], v[32:47]\n\t" \
    "s_add_u32 s63, s63, 4\n\t" \
    "buffer_load_dword v171, v170, s[88:91], s82 offen\n\t" \
    "s_add_u32 s82, s82, 147456\n\t" \
    "v_mfma_f32_32x32x16_bf16 v[48:63], v[112:115], v[132:135], v[48:63]\n\t" \
    "buffer_load_dwordx4 v[132:135], v153, s[48:51], s60 offen sc1\n\t" \
    "s_add_u32 s60, s60, 2048\n\t" \
    "s_waitcnt vmcnt(15) lgkmcnt(0)\n\t" \
    "v_mfma_f32_32x32x16_bf16 v[0:15], v[80:83], v[136:139], v[0:15]\n\t" \
    "ds_add_u32 v161, v155\n\t" \
    "ds_read_b128 v[164:167], v154\n\t" \
    "ds_read_b64_tr_b16 v[100:101], v152 offset:19456\n\t" \
    "ds_read_b64_tr_b16 v[102:103], v152 offset:19968\n\t" \
    "v_mfma_f32_32x32x16_bf16 v[16:31], v[84:87], v[136:139], v[16:31]\n\t" \
    "ds_read_b64_tr_b16 v[104:105], v152 offset:21504\n\t" \
    "ds_read_b64_tr_b16 v[106:107], v152 offset:22016\n\t" \
    "ds_read_b64_tr_b16 v[108:109], v152 offset:23552\n\t" \
    "ds_read_b64_tr_b16 v[110:111], v152 offset:24064\n\t" \
    "ds_read_b64_tr_b16 v[112:113], v152 offset:25600\n\t" \
    "ds_read_b64_tr_b16 v[114:115], v152 offset:26112\n\t" \
    "v_mfma_f32_32x32x16_bf16 v[32:47], v[88:91], v[136:139], v[32:47]\n\t" \
    "s_setprio 3\n\t" \
    "s_waitcnt lgkmcnt(8)\n\t" \
    "v_readfirstlane_b32 s66, v167\n\t" \
    ".Lga2_%=:\n\t" \
    "s_cmp_ge_u32 s66, s63\n\t" \
    "s_cbranch_scc0 .Lgw2_%=\n\t" \
    ".Lgc2_%=:\n\t" \
    "s_setprio 0\n\t" \
    "v_mfma_f32_32x32x16_bf16 v[48:63], v[92:95], v[136:139], v[48:63]\n\t" \
    "s_add_u32 m0, s61, 0\n\t" \
    "s_nop 0\n\t" \
    "buffer_load_dwordx4 v159, s[40:43], s56 offen lds\n\t" \
    "s_add_u32 m0, s61, 1024\n\t" \
    "s_add_u32 s56, s56, s78\n\t" \
    "buffer_load_dwordx4 v159, s[40:43], s57 offen lds\n\t" \
    "v_mfma_f32_32x32x16_bf16 v[64:79], v[96:99], v[136:139], v[64:79]\n\t" \
    "s_add_u32 m0, m0, s69\n\t" \
    "s_add_u32 s57, s57, s78\n\t" \
    "s_mov_b64 exec, 0xffff\n\t" \
    "buffer_load_dwordx4 v158, s[44:47], s58 offen lds\n\t" \
    "s_mov_b64 exec, -1\n\t" \
    "s_add_u32 s58, s58, s78\n\t" \
    "buffer_load_dwordx4 v[136:139], v153, s[48:51], s59 offen sc1\n\t" \
    "s_add_u32 s59, s59, 2048\n\t" \
    "s_waitcnt lgkmcnt(0)\n\t" \
    "v_mfma_f32_32x32x16_bf16 v[0:15], v[100:103], v[140:143], v[0:15]\n\t" \
    "ds_read_b64_tr_b16 v[80:81], v152 offset:27648\n\t" \
    "ds_read_b64_tr_b16 v[82:83], v152 offset:28160\n\t" \
    "ds_read_b64_tr_b16 v[84:85], v152 offset:29696\n\t" \
    "ds_read_b64_tr_b16 v[86:87], v152 offset:30208\n\t" \
    "v_mfma_f32_32x32x16_bf16 v[16:31], v[104:107], v[140:143], v[16:31]\n\t" \
    "ds_read_b64_tr_b16 v[88:89], v152 offset:31744\n\t" \
    "ds_read_b64_tr_b16 v[90:91], v152 offset:32256\n\t" \
    "ds_read_b64_tr_b16 v[92:93], v152 offset:33792\n\t" \
    "ds_read_b64_tr_b16 v[94:95], v152 offset:34304\n\t" \
    "ds_read_b64_tr_b16 v[96:97], v152 offset:35840\n\t" \
    "ds_read_b64_tr_b16 v[98:99], v152 offset:36352\n\t" \
    "v_mfma_f32_32x32x16_bf16 v[32:47], v[108:111], v[140:143], v[32:47]\n\t" \
    "s_cmp_lg_u32 s65, 0\n\t" \
    "s_cbranch_scc1 .Lgk3_%=\n\t" \
    ".Lgk4_%=:\n\t" \
    "s_sub_u32 s68, s68, 1\n\t" \
    "v_mfma_f32_32x32x16_bf16 v[48:63], v[112:115], v[140:143], v[48:63]\n\t" \
    "buffer_load_dwordx4 v[140:143], v153, s[48:51], s60 offen sc1\n\t" \
    "s_add_u32 s60, s60, 2048\n\t" \
    "s_waitcnt vmcnt(15) lgkmcnt(0)\n\t" \
    "v_mfma_f32_32x32x16_bf16 v[0:15], v[80:83], v[144:147], v[0:15]\n\t" \
    "ds_add_u32 v162, v155\n\t" \
    "ds_read_b128 v[164:167], v154\n\t" \
    "ds_read_b64_tr_b16 v[100:101], v152 offset:28672\n\t" \
    "ds_read_b64_tr_b16 v[102:103], v152 offset:29184\n\t" \
    "v_mfma_f32_32x32x16_bf16 v[16:31], v[84:87], v[144:147], v[16:31]\n\t" \
    "ds_read_b64_tr_b16 v[104:105], v152 offset:30720\n\t" \
    "ds_read_b64_tr_b16 v[106:107], v152 offset:31232\n\t" \
    "ds_read_b64_tr_b16 v[108:109], v152 offset:32768\n\t" \
    "ds_read_b64_tr_b16 v[110:111], v152 offset:33280\n\t" \
    "ds_read_b64_tr_b16 v[112:113], v152 offset:34816\n\t" \
    "ds_read_b64_tr_b16 v[114:115], v152 offset:35328\n\t" \
    "v_mfma_f32_32x32x16_bf16 v[32:47], v[88:91], v[144:147], v[32:47]\n\t" \
    "s_setprio 3\n\t" \
    "s_waitcnt lgkmcnt(8)\n\t" \
    "v_readfirstlane_b32 s66, v164\n\t" \
    "v_add_u32 v163, s76, v152\n\t" \
    ".Lga3_%=:\n\t" \
    "s_cmp_ge_u32 s66, s63\n\t" \
    "s_cbranch_scc0 .Lgw3_%=\n\t" \
    ".Lgc3_%=:\n\t" \
    "s_setprio 0\n\t" \
    "v_mfma_f32_32x32x16_bf16 v[48:63], v[92:95], v[144:147], v[48:63]\n\t" \
    "s_add_u32 m0, s61, 9216\n\t" \
    "s_nop 0\n\t" \
    "buffer_load_dwordx4 v159, s[40:43], s56 offen lds\n\t" \
    "s_add_u32 m0, s61, 10240\n\t" \
    "s_add_u32 s56, s56, s78\n\t" \
    "buffer_load_dwordx4 v159, s[40:43], s57 offen lds\n\t" \
    "v_mfma_f32_32x32x16_bf16 v[64:79], v[96:99], v[144:147], v[64:79]\n\t" \
    "s_add_u32 m0, m0, s69\n\t" \
    "s_add_u32 s57, s57, s78\n\t" \
    "s_mov_b64 exec, 0xffff\n\t" \
    "buffer_load_dwordx4 v158, s[44:47], s58 offen lds\n\t" \
    "s_mov_b64 exec, -1\n\t" \
    "s_add_u32 s58, s58, s78\n\t" \
    "buffer_load_dwordx4 v[144:147], v153, s[48:51], s59 offen sc1\n\t" \
    "s_add_u32 s59, s59, 2048\n\t" \
    "s_waitcnt lgkmcnt(0)\n\t" \
    "v_mfma_f32_32x32x16_bf16 v[0:15], v[100:103], v[148:151], v[0:15]\n\t" \
    "ds_read_b64_tr_b16 v[80:81], v163 offset:0\n\t" \
    "ds_read_b64_tr_b16 v[82:83], v163 offset:512\n\t" \
    "ds_read_b64_tr_b16 v[84:85], v163 offset:2048\n\t" \
    "ds_read_b64_tr_b16 v[86:87], v163 offset:2560\n\t" \
    "v_mfma_f32_32x32x16_bf16 v[16:31], v[104:107], v[148:151], v[16:31]\n\t" \
    "ds_read_b64_tr_b16 v[88:89], v163 offset:4096\n\t" \
    "ds_read_b64_tr_b16 v[90:91], v163 offset:4608\n\t" \
    "ds_read_b64_tr_b16 v[92:93], v163 offset:6144\n\t" \
    "ds_read_b64_tr_b16 v[94:95], v163 offset:6656\n\t" \
    "ds_read_b64_tr_b16 v[96:97], v163 offset:8192\n\t" \
    "ds_read_b64_tr_b16 v[98:99], v163 offset:8704\n\t" \
    "v_mfma_f32_32x32x16_bf16 v[32:47], v[108:111], v[148:151], v[32:47]\n\t" \
    "s_add_u32 s63, s63, 4\n\t" \
    "s_mov_b32 s66, s61\n\t" \
    "s_mov_b32 s61, s62\n\t" \
    "s_mov_b32 s62, s66\n\t" \
    "s_sub_u32 s76, 0, s76\n\t" \
    "v_mfma_f32_32x32x16_bf16 v[48:63], v[112:115], v[148:151], v[48:63]\n\t" \
    "buffer_load_dwordx4 v[148:151], v153, s[48:51], s60 offen sc1\n\t" \
    "s_add_u32 s60, s60, 2048\n\t" \
    "s_setprio 3\n\t" \
    "v_mov_b32 v152, v163\n\t" \
    "s_setprio 0\n\t" \
    "s_cmp_gt_u32 s68, 0\n\t" \
    "s_cbranch_scc1 .Lg1_%=\n\t" \
    "s_branch .Lg9_%=\n\t" \
    ".Lgw0_%=:\n\t" \
    "s_sub_u32 s77, s77, 1\n\t" \
    "s_cmp_lt_i32 s77, 0\n\t" \
    "s_cbranch_scc1 .Lgc0_%=\n\t" \
    "s_sleep 1\n\t" \
    "ds_read_b128 v[164:167], v154\n\t" \
    "s_waitcnt lgkmcnt(0)\n\t" \
    "v_readfirstlane_b32 s66, v167\n\t" \
    "s_branch .Lga0_%=\n\t" \
    ".Lgw1_%=:\n\t" \
    "s_sub_u32 s77, s77, 1\n\t" \
    "s_cmp_lt_i32 s77, 0\n\t" \
    "s_cbranch_scc1 .Lgc1_%=\n\t" \
    "s_sleep 1\n\t" \
    "ds_read_b128 v[164:167], v154\n\t" \
    "s_waitcnt lgkmcnt(0)\n\t" \
    "v_readfirstlane_b32 s66, v164\n\t" \
    "s_branch .Lga1_%=\n\t" \
    ".Lgw2_%=:\n\t" \
    "s_sub_u32 s77, s77, 1\n\t" \
    "s_cmp_lt_i32 s77, 0\n\t" \
    "s_cbranch_scc1 .Lgc2_%=\n\t" \
    "s_sleep 1\n\t" \
    "ds_read_b128 v[164:167], v154\n\t" \
    "s_waitcnt lgkmcnt(0)\n\t" \
    "v_readfirstlane_b32 s66, v167\n\t" \
    "s_branch .Lga2_%=\n\t" \
    ".Lgw3_%=:\n\t" \
    "s_sub_u32 s77, s77, 1\n\t" \
    "s_cmp_lt_i32 s77, 0\n\t" \
    "s_cbranch_scc1 .Lgc3_%=\n\t" \
    "s_sleep 1\n\t" \
    "ds_read_b128 v[164:167], v154\n\t" \
    "s_waitcnt lgkmcnt(0)\n\t" \
    "v_readfirstlane_b32 s66, v164\n\t" \
    "s_branch .Lga3_%=\n\t" \
    ".Lgk1_%=:\n\t" \
    "s_mul_i32 s66, s64, 19456\n\t" \
    "s_and_b32 s67, s64, 3\n\t" \
    "s_mul_i32 s67, s67, 19456\n\t" \
    "s_add_u32 s67, s67, s71\n\t" \
    "s_add_u32 m0, s67, 0\n\t" \
    "s_add_u32 s65, s66, 0\n\t" \
    "buffer_load_dwordx4 v153, s[52:55], s65 offen lds\n\t" \
    "s_add_u32 m0, s67, 4096\n\t" \
    "s_add_u32 s65, s66, 4096\n\t" \
    "buffer_load_dwordx4 v153, s[52:55], s65 offen lds\n\t" \
    "s_add_u32 m0, s67, 8192\n\t" \
    "s_add_u32 s65, s66, 8192\n\t" \
    "buffer_load_dwordx4 v153, s[52:55], s65 offen lds\n\t" \
    "s_add_u32 m0, s67, 12288\n\t" \
    "s_add_u32 s65, s66, 12288\n\t" \
    "buffer_load_dwordx4 v153, s[52:55], s65 offen lds\n\t" \
    "s_add_u32 m0, s67, s70\n\t" \
    "s_add_u32 s65, s66, s70\n\t" \
    "buffer_load_dwordx4 v153, s[52:55], s65 offen lds\n\t" \
    "s_add_u32 s64, s64, 1\n\t" \
    "s_add_u32 s74, s74, 4\n\t" \
    "s_cmp_eq_u32 s64, 8\n\t" \
    "s_cselect_b32 s75, 16, 8\n\t" \
    "s_branch .Lgk2_%=\n\t" \
    ".Lgk3_%=:\n\t" \
    "s_setprio 3\n\t" \
    "v_lshlrev_b32 v160, 4, v160\n\t" \
    "s_setprio 0\n\t" \
    "s_mov_b32 s65, 0\n\t" \
    "s_waitcnt vmcnt(10)\n\t" \
    "s_mov_b64 exec, 1\n\t" \
    "ds_add_u32 v154, v160 offset:8\n\t" \
    "s_mov_b64 exec, -1\n\t" \
    "s_branch .Lgk4_%=\n\t" \
    ".Lg9_%=:\n\t" \
    "s_setprio 3\n\t" \
    "s_waitcnt vmcnt(0) lgkmcnt(0)\n\t" \
    "s_waitcnt lgkmcnt(0)\n\t" \
    "s_nop 7\n\t" \
    "s_nop 7\n\t" \
    ""
#define K2_GCLOB "v80", "v81", "v82", "v83", "v84", "v85", "v86", "v87", "v88", "v89", "v90", "v91", "v92", "v93", "v94", "v95", "v96", "v97", "v98", "v99", "v100", "v101", "v102", "v103", "v104", "v105", "v106", "v107", "v108", "v109", "v110", "v111", "v112", "v113", "v114", "v115", "v116", "v117", "v118", "v119", "v120", "v121", "v122", "v123", "v124", "v125", "v126", "v127", "v128", "v129", "v130", "v131", "v132", "v133", "v134", "v135", "v136", "v137", "v138", "v139", "v140", "v141", "v142", "v143", "v144", "v145", "v146", "v147", "v148", "v149", "v150", "v151", "v164", "v165", "v166", "v167", "v168", "v169"


__global__ __launch_bounds__(512, 2) void k1_main(const u16* __restrict__ w1, const u16* __restrict__ wq, const u16* __restrict__ b1, const u16* __restrict__ w2,
                                                  const u16* __restrict__ x, const u16* __restrict__ b2, char* __restrict__ ws, u16* __restrict__ out) {
  __shared__ __attribute__((aligned(16))) char lds[K1_LDS];
  asm volatile("" :: "s"(w1), "s"(wq), "s"(b1), "s"(w2), "s"(x), "s"(b2), "s"(ws), "s"(out));
  const int wave8 = __builtin_amdgcn_readfirstlane(threadIdx.x >> 6), wave = wave8 & 3;
  const int xcd = blockIdx.x & 7, idx = blockIdx.x >> 3;
  const int mg = (xcd & 1) * 8 + (idx >> 2), kq = xcd >> 1, cu = idx & 3, g = kq * 4 + cu;
  const int mt = mg * 4 + wave;
  const int tS = (g & 1) ? 9 * g : 9 * g + 8, tP = (g & 1) ? 9 * g + 1 : 9 * g;
  const u32 ldsb = (u32)(uintptr_t)(LAS char*)lds;
  GAS u32* cnt = (GAS u32*)(ws + WS_CNT + (mg * 4 + kq) * 256);
  if (wave8 < 4) {
    f32x16 QA, QB;
    {
      const int lane = lane_id(), h = lane >> 5;
      u32x4 VV = {ldsb + h * 512 + ((lane & 15) >> 2) * 64 + ((lane >> 4) & 1) * 32 + (lane & 3) * 8, ldsb + h * 1024 + ((lane & 15) >> 2) * 64 + ((lane >> 4) & 1) * 32 + (lane & 3) * 8, ldsb + P1_BIAS + h * 16, (u32)lane * 16};
      u32x16 SA;
      const u32x4 srdh = mksrd(ws + WS_HF + (size_t)mt * 288 * 1024, tS * 2048 + 2048), srdx = mksrd(ws + WS_XF + (size_t)mt * 36 * 1024, 36 * 1024);
#pragma unroll
      for (int i = 0; i < 16; i++) SA[i] = i < 4 ? srdh[i] : (i >= 8 && i < 12) ? srdx[i - 8] : 0u;
      SA[4] = RFL(tS * 2048); SA[5] = 288 * 1024; SA[7] = RFL(tP * 2048);
      asm volatile("s_nop 4\n\t" P1_MAIN
        : "={v[176:191]}"(QA), "={v[192:207]}"(QB), "+{v[246:249]}"(VV), "+{s[64:79]}"(SA)
        :
        : P1_CLOB, "vcc", "scc", "memory");
    }
    const int lane = lane_id(), r = lane & 31, h = lane >> 5;
    u32x4 qm[2]; u32 qt[2];
    { auto qs = [&](float a, float b) -> u32 { return pk(rbf(a) * QSCALE, rbf(b) * QSCALE); };
      qm[0] = (u32x4){qs(QA[0], QA[1]), qs(QA[2], QA[3]), qs(QA[4], QA[5]), qs(QA[6], QA[7])};
      qm[1] = (u32x4){qs(QB[0], QB[1]), qs(QB[2], QB[3]), qs(QB[4], QB[5]), qs(QB[6], QB[7])};
      qt[0] = h == 0 ? qs(QA[8], QA[9]) : 0u; qt[1] = h == 0 ? qs(QB[8], QB[9]) : 0u; }
    const int vr = r < 19 ? r : (r == 22 ? 18 : 19);
    const int koff = lane * 16, toff = 4096 + r * 16, voff = 4608 + (h * 20 + vr) * 16;
    f32x16 zero;
#pragma unroll
    for (int i = 0; i < 16; i++) zero[i] = 0.f;
    f32x16 oA, oB, s0A, s0B, s1A, s1B;
    float mA, mB;
    auto set_ref = [&](float want, u32& qtail) -> float {
      const float hi = rbf(want), lo = rbf(want - hi);
      if (h == 1) qtail = pk(-hi, -lo);
      return hi + lo;
    };
    u32x4 pA0, pA1, pB0, pB1, km, kt, v0, v1;
    const u32 tpa = ldsb + (h == 0 ? toff : K2_FL);
    km = *(LAS const u32x4*)(lds + koff); kt = *(LAS const u32x4*)(uintptr_t)tpa;
    {
      s0A = mfma32(kt, (u32x4){qt[0], 0u, 0u, 0u}, mfma32(km, qm[0], zero)); s0B = mfma32(kt, (u32x4){qt[1], 0u, 0u, 0u}, mfma32(km, qm[1], zero));
      float a = s0A[0], b = s0B[0];
#pragma unroll
      for (int i = 1; i < 16; i++) { a = fmaxf(a, s0A[i]); b = fmaxf(b, s0B[i]); }
      a = fmaxf(a, __shfl_xor(a, 32)); b = fmaxf(b, __shfl_xor(b, 32));
      mA = set_ref(ceilf(a) + M_MARGIN, qt[0]); mB = set_ref(ceilf(b) + M_MARGIN, qt[1]);
#pragma unroll
      for (int i = 0; i < 16; i++) { s0A[i] -= mA; s0B[i] -= mB; }
    }
    km = *(LAS const u32x4*)(lds + koff + 1024);
    pA0 = (u32x4){0u, 0u, 0u, 0u}; pA1 = pA0; pB0 = pA0; pB1 = pA0; v0 = pA0; v1 = pA0;
    oA = zero; oB = zero;
    int t = 0, spin = 0x40000;
    u32x10 AD = {ldsb + koff, tpa, ldsb + voff, (u32)(1 - h), ldsb + K2_FL + 24, 0u, ldsb + K2_FL + (lane == 0 ? 20 : 256 + lane * 4), 0x20000u, 0u, 0u};
    __builtin_amdgcn_s_setprio(2);
    while (true) {
      u32x8 QT = {qt[0], 0u, 0u, qt[0], qt[1], 0u, 0u, qt[1]};
      asm volatile(K2_ALOOP
          : "+{v[0:15]}"(oA), "+{v[16:31]}"(oB), "+{v[32:47]}"(s0A), "+{v[48:63]}"(s0B), "=&{v[64:79]}"(s1A), "=&{v[80:95]}"(s1B),
            "+{v[96:99]}"(pA0), "+{v[100:103]}"(pA1), "+{v[104:107]}"(pB0), "+{v[108:111]}"(pB1),
            "+{v[120:123]}"(km), "+{v[124:127]}"(kt), "+{v[128:131]}"(v0), "+{v[132:135]}"(v1), "+{v[144:153]}"(AD), [t] "+s"(t), [spin] "+s"(spin)
          : "{v[112:115]}"(qm[0]), "{v[116:119]}"(qm[1]), "{v[136:143]}"(QT), [trig] "s"(L_TRIG)
          : "s40", "s41", "s42", "vcc", "scc", "memory");
      if (t >= 64) break;
      oA = mfma32(v0, pA0, oA); oB = mfma32(v0, pB0, oB); oA = mfma32(v1, pA1, oA); oB = mfma32(v1, pB1, oB);
      pA0 = (u32x4){0u, 0u, 0u, 0u}; pA1 = pA0; pB0 = pA0; pB1 = pA0;
      const float nA = set_ref(mA + fmaxf(floorf(__builtin_amdgcn_logf(oA[10])) + M_MARGIN, 0.f), qt[0]);
      const float nB = set_ref(mB + fmaxf(floorf(__builtin_amdgcn_logf(oB[10])) + M_MARGIN, 0.f), qt[1]);
      const float dA = nA - mA, dB = nB - mB;
      const int eA = -(int)dA, eB = -(int)dB;
#pragma unroll
      for (int i = 0; i < 16; i++) { oA[i] = __builtin_amdgcn_ldexpf(oA[i], eA); oB[i] = __builtin_amdgcn_ldexpf(oB[i], eB); s0A[i] -= dA; s0B[i] -= dB; }
      mA = nA; mB = nB;
    }
    if (lane == 0) *(volatile LAS u32*)(lds + K2_FL + 32) = 1u;
    oA = mfma32(v0, pA0, oA); oB = mfma32(v0, pB0, oB); oA = mfma32(v1, pA1, oA); oB = mfma32(v1, pB1, oB);
    {
      float tA = 0.f, tB = 0.f;
#pragma unroll
      for (int i = 0; i < 11; i++) { tA += fabsf(oA[i]); tB += fabsf(oB[i]); }
      const bool ok = tA < 3.0e38f && tB < 3.0e38f && oA[10] > 1.0e-30f && oB[10] > 1.0e-30f;
      if (__builtin_expect(!__all(ok), 0)) {
        const char* gk = ws + WS_KV;
        const u32x4 qa = {h == 0 ? qt[0] : 0u, 0u, 0u, 0u}, qb4 = {h == 0 ? qt[1] : 0u, 0u, 0u, 0u};
        float a = -INFINITY, b = -INFINITY;
        for (int pass = 0; pass < 2; pass++) {
          oA = zero; oB = zero;
          for (int tt = 0; tt < 64; tt++) {
            const char* gp = gk + (tt >> 2) * KVG;
            km = *(const u32x4*)(gp + (tt & 3) * 1024 + koff);
            kt = (u32x4){*(const u32*)(gp + toff + (tt & 3) * 4), 0u, 0u, 0u};
            s0A = mfma32(kt, qa, mfma32(km, qm[0], zero)); s0B = mfma32(kt, qb4, mfma32(km, qm[1], zero));
            if (pass == 0) {
#pragma unroll
              for (int i = 0; i < 16; i++) { a = fmaxf(a, s0A[i]); b = fmaxf(b, s0B[i]); }
            } else {
              v0 = *(const u32x4*)(gp + (tt & 3) * 1280 + voff); v1 = *(const u32x4*)(gp + (tt & 3) * 1280 + voff + 640);
#pragma unroll
              for (int i = 0; i < 16; i++) { s0A[i] = __builtin_amdgcn_exp2f(s0A[i] - a); s0B[i] = __builtin_amdgcn_exp2f(s0B[i] - b); }
#pragma unroll
              for (int i = 0; i < 4; i++) { pA0[i] = pk(s0A[2 * i], s0A[2 * i + 1]); pA1[i] = pk(s0A[8 + 2 * i], s0A[9 + 2 * i]); pB0[i] = pk(s0B[2 * i], s0B[2 * i + 1]); pB1[i] = pk(s0B[8 + 2 * i], s0B[9 + 2 * i]); }
              oA = mfma32(v0, pA0, oA); oB = mfma32(v0, pB0, oB); oA = mfma32(v1, pA1, oA); oB = mfma32(v1, pB1, oB);
            }
          }
          if (pass == 0) { a = fmaxf(a, __shfl_xor(a, 32)); b = fmaxf(b, __shfl_xor(b, 32)); }
        }
      }
    }
    {
      const float ia = 1.0f / oA[10], ib = 1.0f / oB[10];
      auto po = [&](float a, float b) -> u32 { return min(pk(a, b), 0xfffffffeu); };
      const u32 ob = (u32)WS_OF + (mt * 4 + kq) * 9216;
      st16(ws, ob + (2 * cu) * 1024 + lane * 16, (u32x4){po(oA[0] * ia, oA[1] * ia), po(oA[2] * ia, oA[3] * ia), po(oA[4] * ia, oA[5] * ia), po(oA[6] * ia, oA[7] * ia)});
      st16(ws, ob + (2 * cu + 1) * 1024 + lane * 16, (u32x4){po(oB[0] * ib, oB[1] * ib), po(oB[2] * ib, oB[3] * ib), po(oB[4] * ib, oB[5] * ib), po(oB[6] * ib, oB[7] * ib)});
      if (h == 0) st8(ws, ob + 8192 + ((cu & 1) * 32 + r) * 16 + (cu >> 1) * 8, (u32x2){po(oA[8] * ia, oA[9] * ia), po(oB[8] * ib, oB[9] * ib)});
    }
    __builtin_amdgcn_s_setprio(0);
    {
      const int cp = cu >> 1, u = cu & 1, mf = mg * 4 + kq;
      const __amdgpu_buffer_rsrc_t rs = __builtin_amdgcn_make_buffer_rsrc(ws, 0, 0x7fffffff, 0x00020000);
      float sum[3][8]; u32 oo[3], eo[3];
      u32x4 xv[3], bv[3], gv[13];
#pragma unroll
      for (int i = 0; i < 3; i++) {
        const int c = (i < 2 ? u + wave + 4 * i : 8 - 8 * u) * 2 + h, n = cu * 144 + c * 8;
        oo[i] = (mf * 32 + r) * D + n;
        eo[i] = K2_WBASE + r * 576 + ((2 * c) ^ ((r >> 1) & 3)) * 16;
        xv[i] = *(const u32x4*)(x + oo[i]); bv[i] = *(const u32x4*)(b2 + n);
      }
      GAS u32* cg = (GAS u32*)(ws + WS_CNG + (mg * 2 + cp) * 128);
      _Pragma("unroll 1") for (int sp = 0; sp < 0x40000 && __hip_atomic_load(cg, __ATOMIC_RELAXED, __HIP_MEMORY_SCOPE_AGENT) < 8u; sp++) __builtin_amdgcn_s_sleep(4);
      __builtin_amdgcn_fence(__ATOMIC_ACQUIRE, "wavefront");
      const u32 gs = (u32)WS_G + mf * 2048;
#pragma unroll
      for (int i = 0; i < 2; i++)
#pragma unroll
        for (int k = 1; k < 4; k++) gv[i * 3 + k - 1] = __builtin_amdgcn_raw_buffer_load_b128(rs, lane * 16, gs + (((kq ^ k) * 4 + cu) * 5 + (wave >> 1) + 2 * i) * (64 * 2048) + (wave & 1) * 1024, 16);
      if (wave == 0) {
#pragma unroll
        for (int k = 1; k < 8; k++) gv[5 + k] = __builtin_amdgcn_raw_buffer_load_b128(rs, lane * 16, gs + (((kq ^ (k >> 1)) * 4 + (cu ^ (k & 1))) * 5 + 4) * (64 * 2048) + u * 1024, 16);
      } else {
#pragma unroll
        for (int k = 1; k < 8; k++) gv[5 + k] = (u32x4){0u, 0u, 0u, 0u};
      }
#pragma unroll
      for (int i = 0; i < 3; i++)
#pragma unroll
        for (int d = 0; d < 4; d++) {
          float lo = bf2f(xv[i][d] & 0xffffu) + bf2f(bv[i][d] & 0xffffu), hi = __uint_as_float(xv[i][d] & 0xffff0000u) + __uint_as_float(bv[i][d] & 0xffff0000u);
#pragma unroll
          for (int k = 0; k < (i < 2 ? 3 : 7); k++) { lo += bf2f(gv[i * 3 + k][d] & 0xffffu); hi += __uint_as_float(gv[i * 3 + k][d] & 0xffff0000u); }
          sum[i][2 * d] = lo; sum[i][2 * d + 1] = hi;
        }
#pragma unroll
      for (int i = 0; i < 3; i++)
#pragma unroll
        for (int d = 0; d < 8; d++) PIN(sum[i][d]);
      LBAR();
#pragma unroll
      for (int i = 0; i < 3; i++) {
        if (i < 2 || wave == 0) {
#pragma unroll
          for (int k = 0; k < 4; k++) {
            const f32x4 a0 = *(LAS const f32x4*)(lds + eo[i] + k * 18432), a1 = *(LAS const f32x4*)(lds + (eo[i] ^ 16) + k * 18432);
#pragma unroll
            for (int d = 0; d < 4; d++) { sum[i][d] += a0[d]; sum[i][4 + d] += a1[d]; }
          }
          *(u32x4*)(out + oo[i]) = (u32x4){pk(sum[i][0], sum[i][1]), pk(sum[i][2], sum[i][3]), pk(sum[i][4], sum[i][5]), pk(sum[i][6], sum[i][7])};
        }
      }
    }
  } else {
    const int lane = lane_id(), r = lane & 31, h = lane >> 5, w = wave;
    {
      const u32 drow = r >> 2, dch = (lane & 3) * 16;
      const u32x4 srdS = mksrd((const char*)w1 + tS * 64, 575 * F * 2 + 64), srdP = mksrd((const char*)w1 + tP * 64, 575 * F * 2 + 512);
      const u32x4 srdQ = mksrd((const char*)wq + g * 72, 576 * D * 2 - g * 72);
      const u32 voS = (lane >> 2) * (F * 2) + dch, voP = drow * (F * 2) + dch + h * 64, voQ = drow * (D * 2) + dch + h * 36;
      const u32 ldsw = ldsb + w * 6144;
      u32 bv0, bv1;
      asm volatile("s_nop 4\n\tglobal_load_ushort %0, %2, %4\n\tglobal_load_ushort %1, %3, %4" : "=&v"(bv0), "=&v"(bv1) : "v"((tP * 32 + w * 64 + lane) * 2), "v"((tS * 32 + r) * 2), "s"(b1) : "memory");
      auto stage = [&](int p, int c, int slot) {
        const u32 dst = ldsw + slot * P1_STAGE, k0 = 192 * c + 48 * w;
        if (p == 0) { _Pragma("unroll") for (int j = 0; j < 3; j++) bdma16(srdS, voS, (k0 + 16 * j) * (F * 2), ldsb + slot * P1_STAGE + (w * 3 + j) * 1024); }
        else if (p < 5) { _Pragma("unroll") for (int j = 0; j < 6; j++) bdma16(srdP, voP, (k0 + 8 * j) * (F * 2) + (p - 1) * 128, dst + j * 1024); }
        else { _Pragma("unroll") for (int j = 0; j < 6; j++) bdma16(srdQ, voQ, (k0 + 8 * j) * (D * 2), dst + j * 1024); }
      };
      stage(0, 0, 0); stage(0, 1, 1); stage(0, 2, 2);
      WAITV(6);
      asm volatile("" : "+v"(bv0), "+v"(bv1));
      ((LAS float*)(lds + P1_BIAS))[64 + w * 64 + lane] = bf2f(bv0);
      if (w == 0) ((LAS float*)(lds + P1_BIAS))[lane] = bf2f(bv1);
      if (w == 1 && lane < 16) ((LAS u32*)(lds + K2_FL))[lane] = lane < 4 ? 0x3f803f80u : (lane == 5 ? 0x8000u : lane == 6 ? 0x444u : 0u);
      if (w == 2 && lane < 4) ((LAS u32*)(lds + K2_FL + KVG))[lane] = 0x3f803f80u;
      LBAR();
      VBAR(3); stage(1, 0, 3);
      VBAR(6); stage(1, 1, 4);
      VBAR(6); stage(1, 2, 5); stage(2, 0, 0);
      VBAR(12); stage(2, 1, 1); stage(2, 2, 2);
      int p = 3, c = 0, slot = 3;
#pragma unroll 1
      for (int i = 4; i < 13; i++) {
        VBAR(18);
        stage(p, c, slot);
        if (++c == 3) { c = 0; p++; }
        if (++slot == 6) slot = 0;
      }
    }
    const u32 d5 = w < 3 ? 16384 : 15360;
    const int cp = cu >> 1, u = cu & 1;
    const __amdgpu_buffer_rsrc_t rs = __builtin_amdgcn_make_buffer_rsrc(ws, 0, 0x7fffffff, 0x00020000);
    const u32 wof = (u32)WS_WOT + (w * 162 + cp * 9) * 1024;
#define WOFF(f) (wof + (((f) / 5) * 18 + ((f) % 5 < 4 ? 5 * u + (f) % 5 : 4)) * 1024)
    f32x16 acc[5];
    u32 kvb, l16;
    {
      u32x16 SA, SB; u32x8 SC; u32x12 VV;
      const u32x4 sw = mksrd(w2 + (size_t)kq * 1152 * D + (cp * 9 + 5 * u + w) * 32, 1151 * D * 2 + 64);
      const u32x4 sm = mksrd(w2 + (size_t)kq * 1152 * D + (cp * 9 + 4) * 32, 1151 * D * 2 + 64);
      const u32x4 sh = mksrd(ws + WS_HF + (size_t)(mt * 288 + kq * 72) * 1024, 72 * 1024);
      const u32x4 sk = mksrd(ws + WS_KV + w * 1024, 16 * KVG);
      const u32 e = RFL(u * 1024), od = RFL(1024 - u * 1024), cur = RFL(ldsb + K2_WBASE + w * 2048);
#pragma unroll
      for (int i = 0; i < 4; i++) { SA[i] = sw[i]; SA[4 + i] = sm[i]; SA[8 + i] = sh[i]; SA[12 + i] = sk[i]; }
      SB = (u32x16){e * 18, od * 18, e * 18, e, od, cur, cur + 36864u, 4u, 0u, 0u, 0u, 0u, 9u, RFL(7168 - 1792 * w), RFL(d5), RFL(ldsb + w * 1024)};
      SC = (u32x8){RFL((u32)(uintptr_t)cnt), RFL((u32)((uintptr_t)cnt >> 32)), 0u, 8u, 36864u, 0x40000u, 2u * 16u * 1152u, 0u};
      VV = (u32x12){ldsb + K2_WBASE + (h * 4 + ((lane & 15) >> 2)) * 64 + ((lane >> 4) & 1) * 32 + (lane & 3) * 8, (u32)lane * 16, ldsb + K2_FL + 16, 1u, 0u, 0u,
                    (u32)((4 * w + (lane >> 2)) * (D * 2) + (lane & 3) * 16), (u32)((lane >> 2) * (D * 2) + (lane & 3) * 16), 0x100u,
                    ldsb + K2_FL + (lane == 0 ? 16 : 256 + lane * 4), ldsb + K2_FL + (lane == 0 ? 28 : 256 + lane * 4), 0u};
      const u32x4 sp = mksrd(w2 + (size_t)kq * 1152 * D, 1152 * D * 2);
      u32x12 SE = {0u, 0u, 0u, (u32)w, 0u, 0u, 0u, 0u, sp[0], sp[1], sp[2], sp[3]};
      u32x2 DM = {(u32)((idx * 4 + w) * (D * 2) + min(lane, 8) * 128), 0u};
      asm volatile("s_nop 4\n\t" K2_GLOOP
          : "={v[0:15]}"(acc[0]), "={v[16:31]}"(acc[1]), "={v[32:47]}"(acc[2]), "={v[48:63]}"(acc[3]), "={v[64:79]}"(acc[4]),
            "+{v[152:163]}"(VV), "+{s[40:55]}"(SA), "+{s[56:71]}"(SB), "+{s[72:79]}"(SC), "+{s[80:91]}"(SE), "+{v[170:171]}"(DM)
          :
          : K2_GCLOB, "vcc", "scc", "m0", "memory");
      kvb = SB[8]; l16 = VV[1];
    }
    if (l16 == 0) __hip_atomic_fetch_add((LAS u32*)(lds + K2_FL + 40), 1u, __ATOMIC_RELAXED, __HIP_MEMORY_SCOPE_WORKGROUP);
#pragma unroll
    for (int i = 4; i >= 0; i--)
      if (i == 4 || w != kq) {
#pragma unroll
        for (int s = 0; s < 2; s++) {
          const auto e0 = __builtin_amdgcn_permlane32_swap(pk(acc[i][8 * s], acc[i][8 * s + 1]), pk(acc[i][8 * s + 4], acc[i][8 * s + 5]), false, false);
          const auto e1 = __builtin_amdgcn_permlane32_swap(pk(acc[i][8 * s + 2], acc[i][8 * s + 3]), pk(acc[i][8 * s + 6], acc[i][8 * s + 7]), false, false);
          __builtin_amdgcn_raw_buffer_store_b128((u32x4){e0[0], e1[0], e0[1], e1[1]}, rs, l16, (u32)WS_G + ((g * 5 + i) * 64 + mt) * 2048 + s * 1024, 16);
        }
      }
    if (w != kq) {
#pragma unroll
      for (int i = 0; i < 5; i++)
#pragma unroll
        for (int e = 0; e < 16; e++) acc[i][e] = 0.f;
    }
    WAITV(0);
    if (l16 == 0 && __hip_atomic_fetch_add((LAS u32*)(lds + K2_FL + 36), 1u, __ATOMIC_RELAXED, __HIP_MEMORY_SCOPE_WORKGROUP) == 3u)
      __hip_atomic_fetch_add((GAS u32*)(ws + WS_CNG + (mg * 2 + cp) * 128), 1u, __ATOMIC_RELAXED, __HIP_MEMORY_SCOPE_AGENT);
    _Pragma("unroll 1") for (int sp = 0; sp < (1 << 20) && *(volatile LAS u32*)(lds + K2_FL + 40) < 4u; sp++) __builtin_amdgcn_s_sleep(1);
#pragma unroll
    for (int f = 27; f < 45; f++) dma16(ws + WOFF(f), l16, ldsb + K2_WBASE + w * 18432 + (f - 27) * 1024);
    u32x4 wr[27];
#pragma unroll
    for (int f = 0; f < 27; f++) wr[f] = __builtin_amdgcn_raw_buffer_load_b128(rs, l16, WOFF(f), 0);
    for (; kvb < 8; kvb++) {
      _Pragma("unroll 1") for (int sp = 0; sp < (1 << 20) && ((*(volatile LAS u32*)(lds + K2_FL + 20) >> (4 * kvb)) & 15u) != 8u; sp++) __builtin_amdgcn_s_sleep(8);
#pragma unroll
      for (int i = 0; i < 5; i++) { const u32 off = w * 1024 + (i < 4 ? i * 4096 : d5); dma16(ws + WS_KV + kvb * K2_BATCH + off, l16, ldsb + (kvb & 3) * K2_BATCH + off); }
      WAITV(0);
      if (l16 == 0) __hip_atomic_fetch_add((LAS u32*)(lds + K2_FL + 24), 1u << (4 * kvb), __ATOMIC_RELAXED, __HIP_MEMORY_SCOPE_WORKGROUP);
    }
    _Pragma("unroll 1") for (int sp = 0; sp < (1 << 20) && *(volatile LAS u32*)(lds + K2_FL + 32) == 0u; sp++) __builtin_amdgcn_s_sleep(4);
    u32x4 of[9];
#pragma unroll 1
    for (int sp = 0; sp < 0x100; sp++) {
#pragma unroll
      for (int i = 0; i < 9; i++) of[i] = __builtin_amdgcn_raw_buffer_load_b128(rs, l16, (u32)WS_OF + ((mg * 4 + kq) * 4 + w) * 9216 + i * 1024, 16);
      u32 mx = 0u;
#pragma unroll
      for (int i = 0; i < 9; i++) mx = max(max(mx, max(of[i][0], of[i][1])), max(of[i][2], of[i][3]));
      if (__all(mx != ~0u)) break;
#pragma unroll 1
      for (int sq = 0; sq < 0x400; sq++) {
        asm volatile("" ::: "memory"); __builtin_amdgcn_s_sleep(2);
        const u32x4 t_ = __builtin_amdgcn_raw_buffer_load_b128(rs, l16 & 512u, (u32)WS_OF + ((mg * 4 + kq) * 4 + w) * 9216 + 8192, 16);
        if (__all(max(max(t_[0], t_[1]), max(t_[2], t_[3])) != ~0u)) break;
      }
    }
    WAITV(0);
    LAS char* mine = (LAS char*)lds + K2_WBASE + w * 18432;
#pragma unroll
    for (int f = 0; f < 45; f++) {
      acc[f % 5] = mfma32(wr[f % 27], of[f / 5], acc[f % 5]);
      if (f < 18) wr[f] = *(LAS const u32x4*)(mine + f * 1024 + l16);
      PIN(acc[f % 5]); SBAR();
    }
    asm volatile("s_waitcnt lgkmcnt(0)" ::: "memory");
    { const u32 m = (l16 >> 4) & 31, hh = l16 >> 9, key = (m >> 1) & 3;
      LAS char* eb = mine + m * 576;
#pragma unroll
      for (int i = 0; i < 4; i++)
#pragma unroll
        for (int o = 0; o < 4; o++) *(LAS f32x4*)(eb + ((u * 4 + i * 8 + (o & 2) * 2) * 16) + ((((o & 1) * 2 + hh) ^ key) * 16)) = (f32x4){acc[i][4 * o], acc[i][4 * o + 1], acc[i][4 * o + 2], acc[i][4 * o + 3]};
#pragma unroll
      for (int o = 0; o < 2; o++) {
        const f32x4 v = u == 0 ? (f32x4){acc[4][4 * o], acc[4][4 * o + 1], acc[4][4 * o + 2], acc[4][4 * o + 3]} : (f32x4){acc[4][8 + 4 * o], acc[4][9 + 4 * o], acc[4][10 + 4 * o], acc[4][11 + 4 * o]};
        *(LAS f32x4*)(eb + (u == 0 ? 512 : 0) + ((o * 2 + hh) ^ key) * 16) = v;
      } }
    LBAR();
  }
  asm volatile(".Lcode_end:");
}

extern "C" void kernel_launch(void* const* d_in, const int* in_sizes, int n_in, void* d_out, int out_size, void* d_ws, size_t ws_size, hipStream_t stream) {
  const u16* x = (const u16*)d_in[0]; const u16* wq = (const u16*)d_in[1]; const u16* wk = (const u16*)d_in[2]; const u16* wv = (const u16*)d_in[3];
  const u16* wo = (const u16*)d_in[4]; const u16* w1 = (const u16*)d_in[6]; const u16* b1 = (const u16*)d_in[7]; const u16* w2 = (const u16*)d_in[8];
  const u16* b2 = (const u16*)d_in[9];
  char* ws = (char*)d_ws;
  k0_prep<<<256, 896, 0, stream>>>(x, wk, wv, w1, wq, wo, ws);
  k1_main<<<256, 512, 0, stream>>>(w1, wq, b1, w2, x, b2, ws, (u16*)d_out);
}
```
